```python
import torch

def _softmax(x, axis=-1):
    return torch.softmax(x, dim=axis)
def _gelu(x):
    return torch.nn.functional.gelu(x, approximate="tanh")
def _silu(x):
    return torch.nn.functional.silu(x)
def _sigmoid(x):
    return torch.sigmoid(x)
def _layernorm(x, axis=-1, eps=1e-5):
    m = x.mean(dim=axis, keepdim=True)
    v = x.var(dim=axis, keepdim=True, unbiased=False)
    return (x - m) / torch.sqrt(v + eps)
def _scatter_add(tgt, idx, src, axis=0):
    return tgt.clone().index_add_(axis, idx, src)
def _segment_sum(src, seg, n):
    out = torch.zeros((n,) + src.shape[1:], dtype=src.dtype)
    return out.index_add_(0, seg, src)
def _pad(x, pw):
    flat = tuple(v for pair in reversed(pw) for v in pair)
    return torch.nn.functional.pad(x, flat)

kernel_name = 'gated_mix__sliding_block__moe_arch_mod__prompt'

def setup_inputs():

    _w = (lambda a: (lambda t: t.to(torch.bfloat16) if t.is_floating_point() else t)(torch.as_tensor(a)))
    def _A_setup():
        B, S, D = 1, 2048, 640
        return {
            "x": _w((lambda *s: torch.randn(*s, dtype=torch.bfloat16) * 0.05)(B, S, D)),
            "wqkv": _w((lambda *s: torch.randn(*s, dtype=torch.bfloat16) * 1.6)(D, 3 * D)),
            "wo": _w((lambda *s: torch.randn(*s, dtype=torch.bfloat16) * 0.05)(D, D)),
        }
    _A_in = _A_setup()

    def _B_setup():
        B, S, D, F, C = 1, 2048, 640, 1536, 8
        return {
            "x": _w((lambda *s: torch.randn(*s, dtype=torch.bfloat16) * 0.05)(B, S, D)),
            "w_r": _w((lambda *s: torch.randn(*s, dtype=torch.bfloat16) * 3.2)(D)),
            "w_gate": _w((lambda *s: torch.randn(*s, dtype=torch.bfloat16) * 0.4)(D, F)),
            "w_up": _w((lambda *s: torch.randn(*s, dtype=torch.bfloat16) * 0.4)(D, F)),
            "w_down": _w((lambda *s: torch.randn(*s, dtype=torch.bfloat16) * 0.4)(F, D)),
        }
    _B_in = _B_setup()

    return {
        "A_x": _A_in["x"],
        "A_wqkv": _A_in["wqkv"],
        "A_wo": _A_in["wo"],
        "B_x": _B_in["x"],
        "B_w_r": _B_in["w_r"],
        "B_w_gate": _B_in["w_gate"],
        "B_w_up": _B_in["w_up"],
        "B_w_down": _B_in["w_down"],
        "gate_w": _w((lambda *s: torch.randn(*s, dtype=torch.bfloat16) * 0.05)(640) * 16.0),
    }

def reference(A_x, A_wqkv, A_wo, B_x, B_w_r, B_w_gate, B_w_up, B_w_down, gate_w):
    def _A_ref(x, wqkv, wo):
        B, S, D = x.shape
        H = 32; Dh = D // H; W = 256
        qkv = (lambda x, *d: x.permute(*d))((x @ wqkv).reshape(B, S, 3, H, Dh), 2, 0, 3, 1, 4)
        q, k, v = qkv[0], qkv[1], qkv[2]
        scores = torch.einsum("bhqd,bhkd->bhqk", q, k) * (Dh ** -0.5)
        idx = torch.arange(S)
        mask = (idx[:, None] - idx[None, :] >= 0) & (idx[:, None] - idx[None, :] < W)
        scores = torch.where(mask[None, None], scores, -1e9)
        p = _softmax(scores, axis=-1)
        o = torch.einsum("bhqk,bhkd->bhqd", p, v)
        return (lambda x, *d: x.permute(*d))(o, 0, 2, 1, 3).reshape(B, S, D) @ wo

    def _B_ref(x, w_r, w_gate, w_up, w_down):
        B, S, D = x.shape
        C = 8
        r = x @ w_r
        idx = torch.argsort(-r, axis=-1)[:, :C]
        idx_e = (lambda x, n, axis: torch.repeat_interleave(x, n, dim=axis))(idx.reshape(B, C, 1), D, axis=2)
        x_sel = (lambda x, i, axis: torch.take_along_dim(x, i, dim=axis))(x, idx_e, axis=1)
        g = _silu(x_sel @ w_gate)
        u = x_sel @ w_up
        y = (g * u) @ w_down
        r_sel = _sigmoid((lambda x, i, axis: torch.take_along_dim(x, i, dim=axis))(r, idx, axis=1))
        y = y * r_sel.reshape(B, C, 1)
        b_off = (torch.arange(B) * S).reshape(B, 1)
        flat_idx = (idx + b_off).reshape(B * C)
        zero = (x * 0.0).reshape(B * S, D)
        upd = _scatter_add(zero, flat_idx, y.reshape(B * C, D), axis=0)
        return x + upd.reshape(B, S, D)

    x = A_x
    g = _sigmoid((x * gate_w).sum(-1, keepdims=True))
    a = _A_ref(x, A_wqkv, A_wo)
    b = _B_ref(x, B_w_r, B_w_gate, B_w_up, B_w_down)
    return g * a + (1 - g) * b
```

```cpp
#include <hip/hip_runtime.h>
#include <hip/hip_bf16.h>
typedef unsigned short u16;
__device__ __forceinline__ float b2f(u16 v){ return __uint_as_float(((unsigned)v)<<16); }
__device__ __forceinline__ u16 f2b(float f){ __hip_bfloat16 h = __float2bfloat16(f); return *(u16*)&h; }
__device__ __forceinline__ float rb(float f){ return b2f(f2b(f)); }
constexpr int S=2048,D=640,F=1536,H=32,DH=20,W=256;

__global__ void k_qkv(const u16* x,const u16* w,u16* qkv){
  int n = blockIdx.x*64+threadIdx.x; int t0 = blockIdx.y*8;
  float acc[8]={0,0,0,0,0,0,0,0};
  for(int k=0;k<D;k++){ float wv=b2f(w[k*1920+n]);
    #pragma unroll
    for(int i=0;i<8;i++) acc[i]+=b2f(x[(t0+i)*D+k])*wv; }
  #pragma unroll
  for(int i=0;i<8;i++) qkv[(t0+i)*1920+n]=f2b(acc[i]);
}
__global__ void k_rg(const u16* x,const u16* wr,const u16* gw,float* r,float* g){
  int t=blockIdx.x*64+threadIdx.x; float a=0,b=0;
  for(int k=0;k<D;k++){ float xv=b2f(x[t*D+k]); a+=xv*b2f(wr[k]); b+=xv*b2f(gw[k]); }
  r[t]=rb(a); float gl=rb(b); g[t]=rb(1.f/(1.f+__expf(-gl)));
}
__global__ void k_attn(const u16* qkv,u16* o){
  int t=blockIdx.x*64+threadIdx.x; int h=blockIdx.y;
  float q[DH];
  #pragma unroll
  for(int d=0;d<DH;d++) q[d]=b2f(qkv[t*1920+h*DH+d]);
  int k0=t-W+1; if(k0<0)k0=0;
  float m=-1e30f;
  for(int j=k0;j<=t;j++){ float s=0;
    #pragma unroll
    for(int d=0;d<DH;d++) s+=q[d]*b2f(qkv[j*1920+640+h*DH+d]);
    s*=0.22360679774997896f;
    m=fmaxf(m,s);}
  float l=0; float acc[DH];
  #pragma unroll
  for(int d=0;d<DH;d++) acc[d]=0;
  for(int j=k0;j<=t;j++){ float s=0;
    #pragma unroll
    for(int d=0;d<DH;d++) s+=q[d]*b2f(qkv[j*1920+640+h*DH+d]);
    s*=0.22360679774997896f;
    float p=__expf(s-m); l+=p; p=rb(p);
    #pragma unroll
    for(int d=0;d<DH;d++) acc[d]+=p*b2f(qkv[j*1920+1280+h*DH+d]); }
  #pragma unroll
  for(int d=0;d<DH;d++) o[t*D+h*DH+d]=f2b(acc[d]/l);
}
__global__ void k_top(const float* r,int* idx,int* slot){
  int t=blockIdx.x*64+threadIdx.x; float v=r[t]; int c=0;
  for(int j=0;j<S;j++){ float u=r[j]; c += (u>v)||(u==v&&j<t); }
  slot[t]= c<8? c:-1; if(c<8) idx[c]=t;
}
__global__ void k_ffn1(const u16* x,const int* idx,const u16* wg,const u16* wu,u16* hb){
  int f=blockIdx.x*64+threadIdx.x; int c=blockIdx.y; int t=idx[c]; float a=0,b=0;
  for(int k=0;k<D;k++){ float xv=b2f(x[t*D+k]); a+=xv*b2f(wg[k*F+f]); b+=xv*b2f(wu[k*F+f]); }
  a=rb(a); b=rb(b); float s=rb(a/(1.f+__expf(-a))); hb[c*F+f]=f2b(s*b);
}
__global__ void k_ffn2(const u16* hb,const int* idx,const float* r,const u16* wd,float* y){
  int n=blockIdx.x*64+threadIdx.x; int c=blockIdx.y; float a=0;
  for(int k=0;k<F;k++) a+=b2f(hb[c*F+k])*b2f(wd[k*D+n]);
  float rs=rb(1.f/(1.f+__expf(-r[idx[c]]))); y[c*D+n]=rb(rb(a)*rs);
}
__global__ void k_out(const u16* o,const u16* wo,const u16* x,const float* g,const int* slot,const float* y,u16* out){
  int n=blockIdx.x*64+threadIdx.x; int t0=blockIdx.y*8;
  float acc[8]={0,0,0,0,0,0,0,0};
  for(int k=0;k<D;k++){ float wv=b2f(wo[k*D+n]);
    #pragma unroll
    for(int i=0;i<8;i++) acc[i]+=b2f(o[(t0+i)*D+k])*wv; }
  #pragma unroll
  for(int i=0;i<8;i++){ int t=t0+i; float a=rb(acc[i]); float b=b2f(x[t*D+n]); int s=slot[t]; if(s>=0) b=rb(b+y[s*D+n]);
    float gg=g[t]; out[t*D+n]=f2b(rb(gg*a)+rb(rb(1.f-gg)*b)); }
}
extern "C" void kernel_launch(void* const* d_in, const int* in_sizes, int n_in, void* d_out, int out_size, void* d_ws, size_t ws_size, hipStream_t stream) {
  const u16* x=(const u16*)d_in[0]; const u16* wqkv=(const u16*)d_in[1]; const u16* wo=(const u16*)d_in[2];
  const u16* wr=(const u16*)d_in[4]; const u16* wg=(const u16*)d_in[5]; const u16* wu=(const u16*)d_in[6]; const u16* wd=(const u16*)d_in[7]; const u16* gw=(const u16*)d_in[8];
  char* ws=(char*)d_ws;
  u16* qkv=(u16*)ws; u16* o=(u16*)(ws+(8<<20)); float* r=(float*)(ws+(12<<20)); float* g=r+2048; int* idx=(int*)(g+2048); int* slot=idx+64; u16* hb=(u16*)(ws+(13<<20)); float* y=(float*)(ws+(14<<20));
  k_qkv<<<dim3(30,256),64,0,stream>>>(x,wqkv,qkv);
  k_rg<<<32,64,0,stream>>>(x,wr,gw,r,g);
  k_attn<<<dim3(32,32),64,0,stream>>>(qkv,o);
  k_top<<<32,64,0,stream>>>(r,idx,slot);
  k_ffn1<<<dim3(24,8),64,0,stream>>>(x,idx,wg,wu,hb);
  k_ffn2<<<dim3(10,8),64,0,stream>>>(hb,idx,r,wd,y);
  k_out<<<dim3(10,256),64,0,stream>>>(o,wo,x,g,slot,y,(u16*)d_out);
}
```

```cpp
#include <hip/hip_runtime.h>
#include <hip/hip_bf16.h>

typedef unsigned short u16;
typedef unsigned int u32;
typedef unsigned long long u64;
typedef __attribute__((ext_vector_type(2))) u32 u32x2;
typedef __attribute__((ext_vector_type(4))) u32 u32x4;
typedef __attribute__((ext_vector_type(4))) float f32x4;
typedef __attribute__((ext_vector_type(8))) __bf16 bf16x8;
typedef __attribute__((ext_vector_type(2))) __bf16 bf16x2;
typedef __attribute__((ext_vector_type(4))) short s16x4;
#define LDSP(T) __attribute__((address_space(3))) T*
typedef __amdgpu_buffer_rsrc_t rsrc_t;
#define DEV __device__ __forceinline__

constexpr u32 WS_QKV = 0;
constexpr u32 WS_O   = WS_QKV + 240u * 32768;
constexpr u32 WS_P1  = WS_O + 80u * 32768;
constexpr u32 WS_Y   = WS_P1 + 480u * 1024;
constexpr u32 WS_FG  = WS_Y + 24u * 8 * 160 * 16;
constexpr u32 WS_FA  = WS_FG + 512u * 128;
constexpr u32 WS_CAND = WS_FA + 512u * 128;
constexpr u32 WS_GG  = WS_CAND + 16u * 128;
constexpr u32 WS_EP  = WS_GG + 2048u * 8;
constexpr u32 WS_END = WS_EP + 128;
constexpr u32 WS_STAMP = WS_END;

constexpr u32 L_X1   = 73728;
constexpr u32 L_F1W  = 51200;
constexpr u32 L_WOB  = 61440;
constexpr u32 L_A0   = 76800;
constexpr u32 L_A1   = 114048;
constexpr u32 L_F2W  = 151296;
constexpr u32 L_RW   = 155648;
constexpr u32 L_CNT  = 159488;
constexpr u32 L_SEL  = 159552;
constexpr u32 L_H    = 159744;
constexpr u32 L_KEYS = 160768;
constexpr u32 L_RB   = 161280;
constexpr u32 L_YS   = 161280;
constexpr u32 LDS_BYTES = 163840;
constexpr int SPIN_MAX = 1 << 14;
constexpr u32 OOB = 0x80000000u;

struct Params {
  const u16* x; const u16* wqkv; const u16* wo; const u16* wr; const u16* wg; const u16* wu; const u16* wd; const u16* gw;
  u16* out; char* ws;
};

#define STAMP(prev, i) do {} while (0)
#define STAMP_ARG
#define STAMP_PASS

DEV rsrc_t mk_rsrc(const void* p, u32 bytes) { return __builtin_amdgcn_make_buffer_rsrc((void*)p, 0, bytes, 0x00020000); }
DEV u32 rfl(u32 v) { return (u32)__builtin_amdgcn_readfirstlane((int)v); }
template <u32 L> DEV void dma(rsrc_t r, u32 voff, u32 soff, u32 lds) {
  asm volatile("s_add_u32 m0, %0, %4\n\ts_nop 0\n\tbuffer_load_dwordx4 %1, %2, %3 offen lds" :: "s"(lds), "v"(voff), "s"(r), "s"(soff), "n"(L) : "memory", "scc");
}
template <u32 L> DEV void dma_sc1(rsrc_t r, u32 voff, u32 soff, u32 lds) {
  asm volatile("s_add_u32 m0, %0, %4\n\ts_nop 0\n\tbuffer_load_dwordx4 %1, %2, %3 offen sc1 lds" :: "s"(lds), "v"(voff), "s"(r), "s"(soff), "n"(L) : "memory", "scc");
}
template <u32 L> DEV void dma3(rsrc_t rX, rsrc_t rW, u32 vX, u32 vW0, u32 vW1, u32 sX, u32 sW, u32 lds) {
  asm volatile("s_add_u32 m0, %0, %8\n\ts_nop 0\n\tbuffer_load_dwordx4 %1, %2, %3 offen lds\n\t"
               "buffer_load_dwordx4 %4, %6, %7 offen offset:1024 lds\n\tbuffer_load_dwordx4 %5, %6, %7 offen offset:2048 lds"
               :: "s"(lds), "v"(vX), "s"(rX), "s"(sX), "v"(vW0), "v"(vW1), "s"(rW), "s"(sW), "n"(L) : "memory", "scc");
}
template <u32 I> DEV void dma_cont(rsrc_t r, u32 voff, u32 soff) {
  asm volatile("buffer_load_dwordx4 %0, %1, %2 offen offset:%3 lds" :: "v"(voff), "s"(r), "s"(soff), "n"(I) : "memory");
}
template <u32 I = 0> DEV void ld128_sc1(u32x4& d, rsrc_t r, u32 voff, u32 soff) { asm volatile("buffer_load_dwordx4 %0, %1, %2, %3 offen offset:%4 sc1" : "=&v"(d) : "v"(voff), "s"(r), "s"(soff), "n"(I) : "memory"); }
template <u32 I = 0> DEV void ld128(u32x4& d, rsrc_t r, u32 voff, u32 soff) { asm volatile("buffer_load_dwordx4 %0, %1, %2, %3 offen offset:%4" : "=&v"(d) : "v"(voff), "s"(r), "s"(soff), "n"(I) : "memory"); }
template <u32 I> DEV void gld128(u32x4& d, const char* a) { asm volatile("global_load_dwordx4 %0, %1, off offset:%2" : "=&v"(d) : "v"(a), "n"(I) : "memory"); }
DEV void ld64_sc1(u32x2& d, rsrc_t r, u32 voff, u32 soff) { asm volatile("buffer_load_dwordx2 %0, %1, %2, %3 offen sc1" : "=&v"(d) : "v"(voff), "s"(r), "s"(soff) : "memory"); }
DEV void ld32_sc1(u32& d, rsrc_t r, u32 voff, u32 soff) { asm volatile("buffer_load_dword %0, %1, %2, %3 offen sc1" : "=&v"(d) : "v"(voff), "s"(r), "s"(soff) : "memory"); }
#define WAITV(n) asm volatile("s_waitcnt vmcnt(%0)" :: "n"(n) : "memory")
template <class T> DEV void pin_(T& x) { asm volatile("" : "+v"(x)); }
#define PIN(x) pin_(x)
#define BAR() do { asm volatile("" ::: "memory"); __builtin_amdgcn_s_waitcnt(0xC07F); asm volatile("s_barrier" ::: "memory"); } while (0)
#define SCHED() __builtin_amdgcn_sched_barrier(0)
DEV void st64_sc1(u32x2 v, rsrc_t r, u32 voff, u32 soff = 0) { __builtin_amdgcn_raw_buffer_store_b64(v, r, voff, soff, 16); }
DEV void st128_sc1(u32x4 v, rsrc_t r, u32 voff) { __builtin_amdgcn_raw_buffer_store_b128(v, r, voff, 0, 16); }
DEV void st32_sc1(u32 v, rsrc_t r, u32 voff) { __builtin_amdgcn_raw_buffer_store_b32(v, r, voff, 0, 16); }

DEV u32 pk(float a, float b) { bf16x2 v = {(__bf16)a, (__bf16)b}; return __builtin_bit_cast(u32, v); }
DEV float rbf(float a) { return (float)(__bf16)a; }
DEV float blo(u32 v) { return __uint_as_float(v << 16); }
DEV float bhi(u32 v) { return __uint_as_float(v & 0xffff0000u); }
DEV f32x4 mfma(u32x4 a, u32x4 b, f32x4 c) { return __builtin_amdgcn_mfma_f32_16x16x32_bf16(__builtin_bit_cast(bf16x8, a), __builtin_bit_cast(bf16x8, b), c, 0, 0, 0); }
DEV u32x2 trrd(char* s, u32 a) { return __builtin_bit_cast(u32x2, __builtin_amdgcn_ds_read_tr16_b64_v4i16((LDSP(s16x4))(s + a))); }
DEV u32x4 lds128(char* s, u32 a) { return *(u32x4*)(s + a); }
DEV u32x4 cat(u32x2 a, u32x2 b) { u32x4 r = {a[0], a[1], b[0], b[1]}; return r; }
DEV float ex2(float v) { return __builtin_amdgcn_exp2f(v); }
DEV float sigmoidf_(float v) { return __builtin_amdgcn_rcpf(1.f + ex2(v * -1.4426950408889634f)); }
DEV u32x4 gran_pack(f32x4 c, u32 tag) {
  const u32 a = __float_as_uint(c[0]), b = __float_as_uint(c[1]), d = __float_as_uint(c[2]), e = __float_as_uint(c[3]);
  u32x4 r = {__builtin_amdgcn_perm(a, e, 0x07060503u), __builtin_amdgcn_perm(b, e, 0x07060502u), __builtin_amdgcn_perm(d, e, 0x07060501u), tag};
  return r;
}
DEV f32x4 gran_unpack(u32x4 g) {
  const u32 t = __builtin_amdgcn_perm(g[0], g[1], 0x04000c0cu);
  f32x4 r = {__uint_as_float(g[0]), __uint_as_float(g[1]), __uint_as_float(g[2]), __uint_as_float(__builtin_amdgcn_perm(t, g[2], 0x0706000cu))};
  return r;
}

DEV void arrive_flag(char* s, int cnt, rsrc_t ws, u32 flagoff, u32 tag, int lane) {
  if (lane == 0) {
    u32 old;
    asm volatile("ds_add_rtn_u32 %0, %1, %2\n\ts_waitcnt lgkmcnt(0)" : "=v"(old) : "v"((u32)(size_t)(LDSP(char))(s + L_CNT + cnt * 4)), "v"(1u) : "memory");
    st32_sc1(tag, ws, old == 7 ? flagoff : OOB);
  }
}
DEV void spin_flags(u32 v, rsrc_t ws, u32 voff, u32 soff, u32 tag) {
#pragma unroll 1
  for (int i = 0; i < SPIN_MAX && !__all(v == tag); ++i) { ld32_sc1(v, ws, voff, soff); WAITV(0); PIN(v); }
}
DEV void spin_gran(u32x2& v, rsrc_t ws, u32 voff, u32 soff, u32 tag) {
#pragma unroll 1
  for (int i = 0; i < SPIN_MAX && !__all(v[1] == tag); ++i) { ld64_sc1(v, ws, voff, soff); WAITV(0); PIN(v); }
}
DEV u32 rank128(char* s, int tid, u32& key) {
  key = *(u32*)(s + L_KEYS + (tid >> 2) * 4);
  u32 c = 0;
#pragma unroll
  for (int i = 0; i < 8; ++i) {
    u32x4 k = lds128(s, L_KEYS + (tid & 3) * 128 + i * 16);
    c += (k[0] > key) + (k[1] > key) + (k[2] > key) + (k[3] > key);
  }
  c += __builtin_amdgcn_update_dpp(0u, c, 0xB1, 0xf, 0xf, true);
  c += __builtin_amdgcn_update_dpp(0u, c, 0x4E, 0xf, 0xf, true);
  return c;
}
DEV void f1w_stage(const Params& p, u32 sb, int w, int lane, int xcd, int loc) {
  const int s32 = xcd * 6 + (loc < 30 ? loc / 5 : 0), kr = loc % 5;
  const u32 vo = (16 * w + (lane >> 2)) * 3072 + (((lane & 3) ^ ((lane >> 5) * 2)) * 16), so = kr * (128 * 3072) + s32 * 64;
  dma<L_F1W>(mk_rsrc(p.wg, 640 * 1536 * 2), vo, so, sb + w * 1024);
  dma<L_F1W + 8192>(mk_rsrc(p.wu, 640 * 1536 * 2), vo, so, sb + w * 1024);
}

DEV void g_epilogue(f32x4 (&acc)[2][2], rsrc_t ws, u32 voff, u32 soff, bool isq) {
  if (isq) {
#pragma unroll
    for (int i = 0; i < 4; ++i) acc[i >> 1][i & 1] *= 0.32259863752095353f;
  }
#pragma unroll
  for (int ft = 0; ft < 2; ++ft)
#pragma unroll
    for (int tt = 0; tt < 2; ++tt) {
      f32x4 a = acc[ft][tt];
      u32x2 v = {pk(a[0], a[1]), pk(a[2], a[3])};
      st64_sc1(v, ws, voff + tt * 256, soff + ft * 65536);
    }
}
constexpr u32 ring_slot(int i) { return (i & 3) == 3 ? L_X1 + 7 * 8192 : (i & 3) * 24576; }
template <int I> struct IC { static constexpr int v = I; };
template <int N, class F> DEV void static_for(F&& f) { if constexpr (N > 0) { static_for<N - 1>(f); f(IC<N - 1>{}); } }

DEV void g_phase(const Params& p, char* s, u32 sb, rsrc_t ws, int w, int lane, int r, int ct, int rot, int xcd, int loc,
                 u32& tag, u32 epv, u32 pf, u32x2& cand, u32 candoff STAMP_ARG) {
  const int cg = w & 3, th = w >> 2, kq = lane >> 4, l15 = lane & 15;
  rsrc_t rW = mk_rsrc((const char*)p.wqkv - 2048, 640 * 3840 + 2048), rX = mk_rsrc(p.x, 2048 * 1280);
  const u32 vW = kq * 3840 + ((l15 ^ (2 * (kq | ((w & 1) << 2)))) * 16),
    vW0 = vW + 1024, vW1 = vW + 4 * 3840;
  const u32 vX = (lane >> 3) * 1280 + (((lane & 7) ^ ((4 * w + kq) & 7)) * 16);
  const u32 sW = 8 * w * 3840 + ct * 256, sX = (64 * r + 8 * w) * 1280, sb3 = sb + w * 3072, sb1 = sb + w * 1024;
  u32 k0 = rot * 128, k1 = rot * 128;
  auto batch = [&](auto ic) { constexpr int i = decltype(ic)::v;
    dma3<ring_slot(i)>(rX, rW, vX, vW0, vW1, sX + k0, sW + k0 * 1920, sb3); k0 = k0 == 1152 ? 0 : k0 + 128; };
  auto x1 = [&](auto jc) { constexpr int j = decltype(jc)::v;
    dma<L_X1 + j * 8192>(rX, vX, sX + k1 + 1024 * 1280, sb1); k1 = k1 == 1152 ? 0 : k1 + 128; };
  STAMP(0, 18);
  batch(IC<0>{});
  asm volatile("s_barrier" ::: "memory");
  batch(IC<1>{});
  asm volatile("s_barrier" ::: "memory");
  batch(IC<2>{});
  STAMP(18, 19);
  const int qp = (lane >> 2) & 3, pp = lane & 3, hrow = qp | ((kq & 1) << 2);
  u32 aW0 = kq * 3072 + 1024 + qp * 256 + (((cg * 4 + (pp >> 1)) ^ (2 * hrow)) * 16) + (pp & 1) * 8, aW1 = aW0 ^ 32;
  const u32 xsw = (kq ^ (l15 >> 1)) * 16;
  u32 aR0 = (4 * th + (l15 >> 3)) * 3072 + (l15 & 7) * 128 + xsw, aR1 = aR0 ^ 64;
  u32 aS0 = L_X1 + (32 * th + l15) * 128 + xsw, aS1 = aS0 ^ 64;
  PIN(aW0); PIN(aW1); PIN(aR0); PIN(aR1); PIN(aS0); PIN(aS1);
  u32x4 wf[20][2];
  f32x4 acc[2][2] = {}, acd[2][2] = {};
  u32x4 xf[2][4], xh[10][4];
  auto mm0 = [&](auto ic, int h) { constexpr int i = decltype(ic)::v;
#pragma unroll
    for (int q = 0; q < 4; ++q) acc[q & 1][q >> 1] = mfma(wf[2 * i + h][q & 1], xf[i & 1][2 * h + (q >> 1)], acc[q & 1][q >> 1]); };
  auto rd1 = [&](int j) {
#pragma unroll
    for (int q = 0; q < 4; ++q) xh[j][q] = lds128(s, ((q >> 1) ? aS1 : aS0) + (j < 6 ? j : j - 6) * 8192 + (q & 1) * 2048); };
  auto mm1 = [&](int j) {
#pragma unroll
    for (int q = 0; q < 8; ++q) acd[q & 1][q >> 1 & 1] = mfma(wf[2 * j + (q >> 2)][q & 1], xh[j][q >> 1], acd[q & 1][q >> 1 & 1]); };
  static_for<10>([&](auto ic) { constexpr int i = decltype(ic)::v;
    constexpr u32 base = (i & 3) == 3 ? 0 : ring_slot(i), jump = (i & 3) == 3 ? ring_slot(3) : (i & 3) == 0 && i ? -ring_slot(3) : 0;
    if constexpr (jump != 0) { aW0 += jump; aW1 += jump; aR0 += jump; aR1 += jump; PIN(aW0); PIN(aW1); PIN(aR0); PIN(aR1); }
    const u32 kx = sX + k0, kw = sW + k0 * 1920;
    k0 = k0 == 1152 ? 0 : k0 + 128;
    WAITV(i < 8 ? 6 : i == 8 ? 5 : 4);
    BAR();
    if constexpr (i < 7) dma<ring_slot(i + 3)>(rX, vX, kx, sb3); else x1(IC<2 * i - 14>{});
    if (i == 0) { PIN(epv); PIN(pf); tag = rfl(epv) * 1664525u + 1013904223u; STAMP(19, 20); }
#pragma unroll
    for (int ks = 0; ks < 2; ++ks) {
      wf[2 * i + ks][0] = cat(trrd(s, base + aW0 + ks * 12288), trrd(s, base + aW0 + ks * 12288 + 1024));
      xf[i & 1][2 * ks] = lds128(s, base + (ks ? aR1 : aR0));
      wf[2 * i + ks][1] = cat(trrd(s, base + aW1 + ks * 12288), trrd(s, base + aW1 + ks * 12288 + 1024));
      xf[i & 1][2 * ks + 1] = lds128(s, base + (ks ? aR1 : aR0) + 6144);
    }
    SCHED();
    if constexpr (i > 0) mm0(IC<i - 1>{}, 0);
    SCHED();
    if constexpr (i < 7) dma_cont<1024>(rW, vW0, kw);
    SCHED();
    if constexpr (i > 0) mm0(IC<i - 1>{}, 1);
    SCHED();
    if constexpr (i < 7) dma_cont<2048>(rW, vW1, kw); else x1(IC<2 * i - 13>{});
  });
  mm0(IC<9>{}, 0); mm0(IC<9>{}, 1);
  STAMP(20, 1);
  const u32 vst = (kq >> 1) * 32768 + l15 * 16 + (kq & 1) * 8, sst = WS_QKV + (ct * 16 + cg * 4) * 32768 + (64 * r + 32 * th) * 16;
  g_epilogue(acc, ws, vst, sst, ct < 5);
  WAITV(4);
  BAR();
  STAMP(1, 2);
  rd1(0); rd1(1); SCHED();
  x1(IC<6>{}); rd1(2); mm1(0); SCHED();
  x1(IC<7>{}); rd1(3); mm1(1); SCHED();
  x1(IC<8>{}); rd1(4); mm1(2); SCHED();
  x1(IC<9>{}); rd1(5); mm1(3); SCHED();
  aS0 += 6 * 8192; aS1 += 6 * 8192; PIN(aS0); PIN(aS1);
  mm1(4); SCHED();
  WAITV(0);
  arrive_flag(s, 0, ws, WS_FG + (r * 16 + ct) * 128, tag, lane);
  BAR();
  STAMP(2, 21);
  rd1(6); rd1(7);
  if (w < 2) ld64_sc1(cand, ws, candoff, 0);
  mm1(5); SCHED();
  f1w_stage(p, sb, w, lane, xcd, loc);
  rd1(8); mm1(6); SCHED();
  rd1(9); mm1(7); SCHED();
  mm1(8); mm1(9);
  STAMP(21, 3);
  g_epilogue(acd, ws, vst, sst + 1024 * 16, ct < 5);
  WAITV(4);
}

DEV void r_phase(const Params& p, char* s, u32 sb, rsrc_t ws, int w, int lane, int tid, int r, int xcd, int loc,
                 u32& tag, u32 epv, u32 pf, u32x2& cand, u32 candoff STAMP_ARG) {
  const int kq = lane >> 4, l15 = lane & 15;
  rsrc_t rX = mk_rsrc(p.x, 2048 * 1280);
  const u32 vX = (lane >> 3) * 1280 + (((lane & 7) ^ ((4 * w + kq) & 7)) * 16), sX = (64 * r + 8 * w) * 1280, sb1 = sb + w * 1024;
  static_for<9>([&](auto ic) { constexpr int i = decltype(ic)::v; dma<i * 8192>(rX, vX, sX + i * 128, sb1); });
  static_for<10>([&](auto ic) { constexpr int i = decltype(ic)::v; dma<L_X1 + i * 8192>(rX, vX, sX + i * 128 + 1024 * 1280, sb1); });
  if ((w & 1) * 1024 + lane * 16 < 1280) {
    if (w < 2) dma<L_RW>(mk_rsrc(p.wr, 1280), w * 1024 + lane * 16, 0, sb1);
    else if (w < 4) dma<L_RW + 1536 - 2048>(mk_rsrc(p.gw, 1280), (w - 2) * 1024 + lane * 16, 0, sb1);
  }
  u32x4 xa, xb;
  ld128<1152>(xa, rX, (64 * r + 16 * (w & 3) + l15) * 1280 + kq * 16, 0); ld128<1216>(xb, rX, (64 * r + 16 * (w & 3) + l15) * 1280 + kq * 16, 0);
  u32 aw = L_RW + (l15 == 1 ? 1536 : 0) + kq * 16;
  u32 aX0 = ((w >> 2) ? L_X1 : 0) + (16 * (w & 3) + l15) * 128 + ((kq ^ (l15 >> 1)) * 16), aX1 = aX0 ^ 64, aX2 = aX0 + 65536, aX3 = aX1 + 65536;
  PIN(aw); PIN(aX0); PIN(aX1); PIN(aX2); PIN(aX3);
  f32x4 acc = {0.f, 0.f, 0.f, 0.f};
  WAITV(0); BAR();
  STAMP(0, 29);
  PIN(epv); PIN(pf); PIN(xa); PIN(xb); tag = rfl(epv) * 1664525u + 1013904223u;
  if (w >> 2) { xa = lds128(s, 8192 + aX2); xb = lds128(s, 8192 + aX3); }
  u32x4 wf[20], xr[18];
#pragma unroll
  for (int i = 0; i < 9; ++i) {
    wf[2 * i] = lds128(s, aw + i * 128); xr[2 * i] = lds128(s, i < 8 ? i * 8192 + aX0 : aX2);
    wf[2 * i + 1] = lds128(s, aw + i * 128 + 64); xr[2 * i + 1] = lds128(s, i < 8 ? i * 8192 + aX1 : aX3);
  }
  wf[18] = lds128(s, aw + 1152); wf[19] = lds128(s, aw + 1216);
  SCHED();
#pragma unroll
  for (int i = 0; i < 18; ++i) acc = mfma(xr[i], wf[i], acc);
  acc = mfma(xa, wf[18], acc);
  acc = mfma(xb, wf[19], acc);
  if (l15 < 2) *(f32x4*)(s + L_RB + l15 * 512 + (16 * w + 4 * kq) * 4) = acc;
  BAR();
  STAMP(29, 30);
  u32x2 gr;
  if (tid < 128) {
    const int tok = (tid >> 6) * 1024 + 64 * r + (tid & 63);
    const float rv = *(float*)(s + L_RB + tid * 4), gl = *(float*)(s + L_RB + 512 + tid * 4);
    const u32 rb = pk(rv, 0.f) & 0xffffu;
    *(u32*)(s + L_KEYS + tid * 4) = ((rb ^ ((rb & 0x8000u) ? 0xffffu : 0x8000u)) << 16) | (u32)(2047 - tok);
    gr[0] = __float_as_uint(sigmoidf_(gl)); gr[1] = tag;
  }
  BAR();
  u32 key; const u32 rk = rank128(s, tid, key);
  if (rk < 8 && (tid & 3) == 0) { u32x2 c = {key, tag}; st64_sc1(c, ws, WS_CAND + r * 128 + rk * 8); }
  if (tid < 128) st64_sc1(gr, ws, WS_GG + ((tid >> 6) * 1024 + 64 * r + (tid & 63)) * 8);
  f1w_stage(p, sb, w, lane, xcd, loc);
  STAMP(30, 3);
  BAR();
  if (w < 2) ld64_sc1(cand, ws, candoff, 0);
  WAITV(0);
}

template <u32 BUF> DEV void attn_stage(rsrc_t ws, u32 sb1, u32 so, int lane) {
  dma_sc1<BUF>(ws, lane * 16, so, sb1);
  dma_sc1<BUF + 6144>(ws, lane * 16, so + 32768, sb1);
  dma_sc1<BUF + 12288>(ws, lane * 16, so + 65536, sb1);
  dma_sc1<BUF + 18432>(ws, lane * 16, so + 80 * 32768, sb1);
  dma_sc1<BUF + 18432 + 6272>(ws, lane * 16, so + 81 * 32768, sb1);
  dma_sc1<BUF + 18432 + 12544>(ws, lane * 16, so + 82 * 32768, sb1);
}
DEV float xmax(float m) {
  auto a = __builtin_amdgcn_permlane16_swap(__float_as_uint(m), __float_as_uint(m), false, false);
  m = fmaxf(__uint_as_float(a[0]), __uint_as_float(a[1]));
  auto b = __builtin_amdgcn_permlane32_swap(__float_as_uint(m), __float_as_uint(m), false, false);
  return fmaxf(__uint_as_float(b[0]), __uint_as_float(b[1]));
}
template <class Mid>
DEV void attn_unit(char* s, u32x4 qf, u32 aK, u32 aV0, u32 aV1, int nskip, int lane, rsrc_t ws, u32 vo0, u32 vo1, u32 so, Mid&& mid) {
  const int kq = lane >> 4, l15 = lane & 15, dg = l15 - 4 * kq;
  const float NEG = -__builtin_inff();
  const f32x4 z = {0.f, 0.f, 0.f, 0.f};
  const u32x2 zz = {0u, 0u};
  u32x4 kf[18], va[9], vb[9];
  kf[16] = lds128(s, aK + 16 * 256); kf[0] = lds128(s, aK); kf[1] = lds128(s, aK + 256);
  va[8] = cat(trrd(s, aV0 + 16 * 256), zz); vb[8] = cat(trrd(s, aV1 + 16 * 256), zz);
  kf[2] = lds128(s, aK + 512); kf[3] = lds128(s, aK + 768);
  va[0] = cat(trrd(s, aV0), trrd(s, aV0 + 256)); vb[0] = cat(trrd(s, aV1), trrd(s, aV1 + 256));
  SCHED();
  f32x4 sd = mfma(kf[16], qf, z);
#pragma unroll
  for (int e = 0; e < 4; ++e) sd[e] = e <= dg ? sd[e] : NEG;
  const float m = xmax(fmaxf(fmaxf(sd[0], sd[1]), fmaxf(sd[2], sd[3])));
  const f32x4 c = {-m, -m, -m, -m};
  f32x4 sa = mfma(kf[0], qf, c), sc = mfma(kf[1], qf, c);
  u32x4 P = {pk(ex2(sd[0] - m), ex2(sd[1] - m)), pk(ex2(sd[2] - m), ex2(sd[3] - m)), 0u, 0u};
  f32x4 o0 = mfma(va[8], P, z), o1 = mfma(vb[8], P, z);
#pragma unroll
  for (int e = 0; e < 4; ++e) sa[e] = e > dg ? sa[e] : NEG;
  static_for<8>([&](auto bc) { constexpr int b = decltype(bc)::v;
    SCHED();
    f32x4 na, nc;
    if (b < 7) na = mfma(kf[2 * b + 2], qf, c);
    if (b < 6) { kf[2 * b + 4] = lds128(s, aK + (2 * b + 4) * 256); kf[2 * b + 5] = lds128(s, aK + (2 * b + 5) * 256); }
    P[0] = pk(ex2(sa[0]), ex2(sa[1])); P[1] = pk(ex2(sa[2]), ex2(sa[3]));
    SCHED();
    if (b < 7) nc = mfma(kf[2 * b + 3], qf, c);
    if (b < 7) {
      va[b + 1] = cat(trrd(s, aV0 + (2 * b + 2) * 256), trrd(s, aV0 + (2 * b + 3) * 256));
      vb[b + 1] = cat(trrd(s, aV1 + (2 * b + 2) * 256), trrd(s, aV1 + (2 * b + 3) * 256));
    }
    P[2] = pk(ex2(sc[0]), ex2(sc[1])); P[3] = pk(ex2(sc[2]), ex2(sc[3]));
    SCHED();
    o0 = mfma(va[b], P, o0);
    o1 = mfma(vb[b], P, o1);
    mid(bc);
    sa = na; sc = nc;
  });
  SCHED();
  auto sw = __builtin_amdgcn_permlane16_swap(__float_as_uint(o1[0]), __float_as_uint(o1[0]), false, false);
  float l = kq == 0 ? __uint_as_float(sw[1]) : o1[0];
  {
    const float big = fmaxf(fmaxf(fmaxf(fabsf(o0[0]), fabsf(o0[1])), fmaxf(fabsf(o0[2]), fabsf(o0[3]))),
                            fmaxf(fmaxf(fabsf(o1[0]), fabsf(o1[1])), fmaxf(fabsf(o1[2]), fabsf(o1[3]))));
    const float sum = (o0[0] + o0[1]) + (o0[2] + o0[3]) + (o1[0] + o1[1]) + (o1[2] + o1[3]);
    const bool ok = l < 1.8446744073709552e19f && big < 1e37f && sum == sum;
    if (__builtin_expect(!__all(ok), 0)) {
      float mt = m;
#pragma unroll 1
      for (int t = nskip; t < 16; ++t) {
        f32x4 v = mfma(lds128(s, aK + t * 256), qf, z);
#pragma unroll
        for (int e = 0; e < 4; ++e) mt = fmaxf(mt, (t > 0 || e > dg) ? v[e] : NEG);
      }
      mt = xmax(mt);
      o0 = z; o1 = z;
#pragma unroll 1
      for (int t = nskip; t < 17; ++t) {
        f32x4 v = mfma(lds128(s, aK + t * 256), qf, z);
#pragma unroll
        for (int e = 0; e < 4; ++e) v[e] = (t == 0 ? e > dg : (t < 16 || e <= dg)) ? ex2(v[e] - mt) : 0.f;
        const u32x4 Q = {pk(v[0], v[1]), pk(v[2], v[3]), 0u, 0u};
        o0 = mfma(cat(trrd(s, aV0 + t * 256), zz), Q, o0);
        o1 = mfma(cat(trrd(s, aV1 + t * 256), zz), Q, o1);
      }
      auto s2 = __builtin_amdgcn_permlane16_swap(__float_as_uint(o1[0]), __float_as_uint(o1[0]), false, false);
      l = kq == 0 ? __uint_as_float(s2[1]) : o1[0];
    }
  }
  const float inv = __builtin_amdgcn_rcpf(l);
  u32x2 v0 = {pk(o0[0] * inv, o0[1] * inv), pk(o0[2] * inv, o0[3] * inv)};
  u32x2 v1 = {pk(o1[0] * inv, o1[1] * inv), pk(o1[2] * inv, o1[3] * inv)};
  st64_sc1(v0, ws, vo0, so);
  st64_sc1(v1, ws, vo1, so);
}

DEV void o_compute(char* s, u32 a, u32x4 (&bw)[5][5], u32x4 (&of)[5]) {
  f32x4 acc[5] = {};
#pragma unroll
  for (int ks = 0; ks < 5; ++ks)
#pragma unroll
    for (int nt = 0; nt < 5; ++nt) acc[nt] = mfma(bw[ks][nt], of[ks], acc[nt]);
#pragma unroll
  for (int nt = 0; nt < 5; ++nt) *(f32x4*)(s + a + nt * 1088) = acc[nt];
}
DEV void o_final(char* s, u32 a, float g, const float (&b)[8], rsrc_t rO, u32 vo, u32 so) {
  f32x4 lo = *(f32x4*)(s + a), hi = *(f32x4*)(s + a + 256);
#pragma unroll
  for (int q = 1; q < 4; ++q) { lo += *(f32x4*)(s + a + q * 10880); hi += *(f32x4*)(s + a + q * 10880 + 256); }
  u32x4 o = {pk(g * lo[0] + b[0], g * lo[1] + b[1]), pk(g * lo[2] + b[2], g * lo[3] + b[3]),
             pk(g * hi[0] + b[4], g * hi[1] + b[5]), pk(g * hi[2] + b[6], g * hi[3] + b[7])};
  __builtin_amdgcn_raw_buffer_store_b128(o, rO, vo, so, 17);
}

__global__ __launch_bounds__(512, 2) void fused_kernel(Params p) {
  extern __shared__ __attribute__((aligned(16))) char smem[];
  char* s = smem;
  const int tid = threadIdx.x, lane = tid & 63;
  u32 pf;
  {
    u64 pc; u32 len;
    asm volatile("s_getpc_b64 %0\nfk_code_lo:\n\ts_mov_b32 %1, fk_code_hi-fk_code_lo" : "=s"(pc), "=s"(len));
    asm volatile("buffer_load_dword %0, %1, %2, 0 offen" : "=&v"(pf) : "v"(tid >= 448 ? (tid - 448 + (blockIdx.x >> 3 & 3) * 64) * 128 : OOB), "s"(mk_rsrc((const void*)pc, len)) : "memory");
  }
  const int w = (int)rfl(tid >> 6);
  const int b = blockIdx.x, xcd = b & 7, loc = b >> 3;
  const u32 sb = rfl((u32)(size_t)(LDSP(char))smem), sb1 = sb + w * 1024;
  rsrc_t ws = mk_rsrc(p.ws, WS_END);
  const int kq = lane >> 4, l15 = lane & 15, qp = (lane >> 2) & 3, pp = lane & 3;
  u32 epv; ld32_sc1(epv, ws, WS_EP, 0);
  if (tid < 16) *(u32*)(s + L_CNT + tid * 4) = 0;
  const int r = (xcd >> 1) * 4 + (loc >> 3), ct = (xcd & 1) * 8 + (loc & 7);
  const int h = xcd * 4 + (loc & 3), gp = loc >> 2;
  const int sl = loc & 7, oi = loc >> 3;
  const int rot = ((loc >> 3) * 10 / 4 + (loc & 7) * 10 / 8 + xcd) % 10;
  u32 tag; u32x2 cand;
  const u32 candoff = WS_CAND + ((tid & 127) >> 3) * 128 + (tid & 7) * 8;
  if (ct < 15) g_phase(p, s, sb, ws, w, lane, r, ct, rot, xcd, loc, tag, epv, pf, cand, candoff STAMP_PASS);
  else r_phase(p, s, sb, ws, w, lane, tid, r, xcd, loc, tag, epv, pf, cand, candoff STAMP_PASS);

  STAMP(3, 22);
  if (w < 2) { PIN(cand); spin_gran(cand, ws, candoff, 0, tag); *(u32*)(s + L_KEYS + tid * 4) = cand[0]; }
  BAR();
  STAMP(22, 4);
  const bool f1 = loc < 30 && w < 4;
  u32x4 fw[4], xg[4];
  { const u32 a = L_F1W + (w >> 1 & 1) * 8192 + (8 * kq + qp) * 64 + (((w ^ kq) & 1) * 32) + pp * 8;
#pragma unroll
    for (int ks = 0; ks < 4; ++ks) fw[ks] = cat(trrd(s, a + ks * 2048), trrd(s, a + ks * 2048 + 256)); }
  { u32 key; const u32 rk = rank128(s, tid, key); if (rk < 8 && (tid & 3) == 0) *(u32*)(s + L_SEL + rk * 4) = key; }
  BAR();
  STAMP(4, 23);
  const u32 selkey = *(u32*)(s + L_SEL + (lane & 7) * 4), seltok = 2047 - (selkey & 0xffffu);
  rsrc_t rX = mk_rsrc(p.x, 2048 * 1280), rWo = mk_rsrc(p.wo, 640 * 1280);
  if (f1) {
    const u32 vo = seltok * 1280 + kq * 16, so = (loc % 5) * 256;
    ld128<0>(xg[0], rX, vo, so); ld128<64>(xg[1], rX, vo, so); ld128<128>(xg[2], rX, vo, so); ld128<192>(xg[3], rX, vo, so);
  }
  const u32 vwa = (lane >> 3) * 1280 + (((lane & 7) ^ (2 * ((kq & 1) | ((w & 1) << 1)))) * 16);
  const u32 vwb = ((lane >> 1 & 19) | (lane >> 2 & 4) | (lane & 8)) * 1280 + 128 + (lane & 1) * 16;
  const u32 swa = w * 10240 + sl * 160, swb = w * 40960 + sl * 160;
  auto wo_round = [&](auto ic) { constexpr int i = decltype(ic)::v;
    if (i < 7 || w < 4) dma<i * 8192>(rWo, vwa, swa + i * 81920, sb1);
    if ((w >> 1) == (i & 3) && (i < 7 || w == 6)) dma<L_WOB + (i >> 2) * 8192>(rWo, vwb, swb + (i >> 2) * 8 * 40960, sb1);
  };
  wo_round(IC<0>{}); wo_round(IC<1>{}); wo_round(IC<2>{});
  const int c0 = (5 * h) >> 1, krow = 2 * gp - 4 + w;
  const u32 fcol = ((c0 + 2 * (lane & 1)) >> 4) * 128;
  const u32 flq = WS_FG + (2 * gp + (w >> 2)) * 2048 + fcol, flk = WS_FG + krow * 2048 + (lane & 2 ? 1280 : 640) + fcol;
  const u32 fl1 = (lane < 4 && w < 6) ? flk : flq, fl0 = krow < 0 ? flq : fl1;
  u32 flv; ld32_sc1(flv, ws, fl0, 0);
  STAMP(23, 24);
  if (f1) WAITV(9); else if (w < 6) WAITV(5); else WAITV(4);
  arrive_flag(s, 1, ws, ct < 15 ? WS_FG + ((16 + r) * 16 + ct) * 128 : OOB, tag, lane);
  STAMP(24, 5);
  if (f1) {
    WAITV(6);
    f32x4 c = {0.f, 0.f, 0.f, 0.f};
#pragma unroll
    for (int ks = 0; ks < 4; ++ks) { PIN(xg[ks]); c = mfma(fw[ks], xg[ks], c); }
    const u32 po = WS_P1 + (((xcd * 6 + loc / 5) * 5 + loc % 5) * 2 + (w >> 1)) * 1024 + (w & 1) * 512 + kq * 128 + l15 * 16;
    st128_sc1(gran_pack(c, tag), ws, l15 < 8 ? po : OOB);
    WAITV(2);
  } else WAITV(1);
  PIN(flv); spin_flags(flv, ws, fl0, 0, tag);
  STAMP(5, 6);
  const u32 skv = WS_QKV + (80 + c0) * 32768 + krow * 1024;
  const u32 vq = (c0 + (kq > 2 ? 2 : kq)) * 32768 + (gp * 128 + w * 16 + l15) * 16;
  u32x4 q0, q1;
  if (w < 6) attn_stage<L_A0>(ws, sb1, krow < 0 ? OOB : skv, lane);
  ld128_sc1(q0, ws, vq, WS_QKV);
  ld32_sc1(flv, ws, fl1, 16 * 2048);
  STAMP(6, 25);
  WAITV(0); PIN(flv); spin_flags(flv, ws, fl1, 16 * 2048, tag);
  STAMP(25, 26);
  if (w < 6) attn_stage<L_A1>(ws, sb1, skv + 16 * 1024, lane);
  ld128_sc1(q1, ws, vq, WS_QKV + 1024 * 16);
  const int odd = h & 1;
  const u32x2 ones = {0x3f803f80u, 0x3f803f80u};
  const u32 aone = 18432 + (odd ? 0 : 12544 + 8) + w * 1024 + lane * 16;
  if (w < 6 && krow >= 0) *(u32x2*)(s + L_A0 + aone) = ones;
  const bool qlo = kq == 3 || (odd && kq == 0), qhi = kq == 3 || (!odd && kq == 2);
  PIN(q0); q0[0] = qlo ? 0u : q0[0]; q0[1] = qlo ? 0u : q0[1]; q0[2] = qhi ? 0u : q0[2]; q0[3] = qhi ? 0u : q0[3];
  const u32 vrow = L_A0 + 18432 + (16 * w + 4 * kq + qp) * 16;
  u32 aK = L_A0 + (kq > 2 ? 2 : kq) * 6144 + w * 256 + l15 * 16;
  u32 aV0 = vrow + ((pp + odd) >> 1) * 6272 + ((pp + odd) & 1) * 8;
  u32 aV1 = vrow + (pp == 0 ? 12544 + odd * 8 : (odd ? 0 : 12544 + 8));
  PIN(aK); PIN(aV0); PIN(aV1);
  u32 vo0 = (c0 + ((kq + odd) >> 1)) * 32768 + ((kq + odd) & 1) * 8 + l15 * 16, vo1 = kq == 0 ? (c0 + 2) * 32768 + odd * 8 + l15 * 16 : OOB;
  const u32 so = WS_O + (gp * 128 + w * 16) * 16;
  PIN(vo0); PIN(vo1);
  BAR();
  STAMP(26, 7);
  attn_unit(s, q0, aK, aV0, aV1, gp < 2 ? (16 - gp * 8 - w > 0 ? 16 - gp * 8 - w : 0) : 0, lane, ws, vo0, vo1, so,
            [&](auto bc) { constexpr int b = decltype(bc)::v; if constexpr (b < 5) wo_round(IC<b + 3>{}); });
  STAMP(7, 8);
  if (w < 4 || w == 6) WAITV(8); else WAITV(7);
  aK += L_A1 - L_A0; aV0 += L_A1 - L_A0; aV1 += L_A1 - L_A0; PIN(aK); PIN(aV0); PIN(aV1);
  if (w < 6) *(u32x2*)(s + L_A1 + aone) = ones;
  PIN(q1); q1[0] = qlo ? 0u : q1[0]; q1[1] = qlo ? 0u : q1[1]; q1[2] = qhi ? 0u : q1[2]; q1[3] = qhi ? 0u : q1[3];
  BAR();
  STAMP(8, 9);
  const int s64 = xcd * 3 + loc / 10, cgp = loc % 10;
  const bool f2 = loc < 30;
  u32x4 pp1[10];
  const u32 vp1 = WS_P1 + (2 * s64 + (w & 1)) * 10240 + lane * 16;
  auto p1load = [&]() { static_for<10>([&](auto ic) { constexpr int i = decltype(ic)::v; ld128_sc1<(i & 3) * 1024>(pp1[i], ws, vp1, (i >> 2) * 4096); }); };
  const int tokA = 128 * xcd + 32 * oi;
  const u32 fa = WS_FA + (xcd * 32 + (lane & 31)) * 128;
  const int tk = (tid * 6554) >> 16, pc = tid - tk * 10;
  u32x4 of[5], xv0, xv1; u32x2 g0, g1;
  const u32 xo = tid < 320 ? (tokA + tk) * 1280 + sl * 160 + pc * 16 : OOB, go = WS_GG + (tokA + (tk & 31)) * 8;
  attn_unit(s, q1, aK, aV0, aV1, 0, lane, ws, vo0, vo1, so + 1024 * 16, [&](auto bc) { constexpr int b = decltype(bc)::v;
    if constexpr (b == 0) dma<L_A0>(rWo, vwa, swa + 60 * 10240, sb1);
    if constexpr (b == 1) { dma<L_A0 + 8192>(rWo, vwa, swa + 68 * 10240, sb1); if (w < 5) dma<L_A0 + 20480>(rWo, vwb, swb + 15 * 40960, sb1); }
    if constexpr (b == 2) { if (w < 4) dma<L_A0 + 16384>(rWo, vwa, swa + 76 * 10240, sb1); if (w >= 6) p1load(); }
    if constexpr (b == 3) dma<L_F2W>(mk_rsrc(p.wd, 1536 * 1280), vwa, (s64 * 64 + 8 * w) * 1280 + cgp * 128, sb1);
    if constexpr (b == 4) {
      if (w < 4) WAITV(5); else if (w == 4) WAITV(4); else if (w == 5) WAITV(3); else WAITV(13);
      arrive_flag(s, 2, ws, WS_FA + (gp * 32 + h) * 128, tag, lane);
    }
    if constexpr (b == 5) { ld128(xv0, rX, xo, 0); ld128(xv1, rX, xo, 1024 * 1280); ld64_sc1(g0, ws, go, 0); ld64_sc1(g1, ws, go, 8192); }
  });
  STAMP(9, 10);
  WAITV(2);
  STAMP(10, 27);
  if (f2 && w >= 6) {
#pragma unroll 1
    for (int it = 0; it < SPIN_MAX; ++it) {
      bool ok = true;
#pragma unroll
      for (int i = 0; i < 10; ++i) { PIN(pp1[i]); ok = ok && pp1[i][3] == tag; }
      if (__all(ok)) break;
      p1load(); WAITV(0);
    }
    f32x4 g = gran_unpack(pp1[0]), u = gran_unpack(pp1[1]);
#pragma unroll
    for (int i = 1; i < 5; ++i) { g += gran_unpack(pp1[2 * i]); u += gran_unpack(pp1[2 * i + 1]); }
    u32x2 hv = {pk(g[0] * sigmoidf_(g[0]) * u[0], g[1] * sigmoidf_(g[1]) * u[1]), pk(g[2] * sigmoidf_(g[2]) * u[2], g[3] * sigmoidf_(g[3]) * u[3])};
    *(u32x2*)(s + L_H + (lane & 7) * 128 + (w & 1) * 64 + (lane >> 3) * 8) = hv;
  }
  BAR();
  STAMP(27, 28);
  const u32 fsw = ((qp >> 1) | ((kq & 1) << 1)) * 32 + pp * 8;
  if (f1) {
    const u32 a = L_F2W + (8 * kq + qp) * 128 + (fsw ^ (w * 32)), ah = L_H + (l15 & 7) * 128 + kq * 16;
    f32x4 c = {0.f, 0.f, 0.f, 0.f};
    c = mfma(cat(trrd(s, a), trrd(s, a + 512)), lds128(s, ah), c);
    c = mfma(cat(trrd(s, a + 4096), trrd(s, a + 4096 + 512)), lds128(s, ah + 64), c);
    st128_sc1(gran_pack(c, tag), ws, l15 < 8 ? WS_Y + ((s64 * 8 + l15) * 160 + cgp * 16 + w * 4 + kq) * 16 : OOB);
    WAITV(1);
  } else WAITV(0);
  arrive_flag(s, 3, ws, WS_FA + ((8 + gp) * 32 + h) * 128, tag, lane);
  ld32_sc1(flv, ws, fa, 0);
  STAMP(28, 11);
  u32x4 bw[5][5];
  { const int qd = w >> 1;
    const u32 a = (qd < 3 ? qd * 20480 : L_A0) + (8 * kq + qp) * 128;
    u32 ab = (qd < 3 ? L_WOB + qd * 5120 : L_A0 + 20480) + ((kq >> 1) * 16 + (kq & 1) * 4 + qp) * 32 + pp * 8, an[4];
    PIN(ab);
#pragma unroll
    for (int nt = 0; nt < 4; ++nt) { an[nt] = a + (fsw ^ (nt * 32)); PIN(an[nt]); }
#pragma unroll
    for (int ks = 0; ks < 5; ++ks) {
#pragma unroll
      for (int nt = 0; nt < 4; ++nt) bw[ks][nt] = cat(trrd(s, an[nt] + ks * 4096), trrd(s, an[nt] + ks * 4096 + 512));
      bw[ks][4] = cat(trrd(s, ab + ks * 1024), trrd(s, ab + ks * 1024 + 256));
    } }
  WAITV(0);
  PIN(flv); spin_flags(flv, ws, fa, 0, tag);
  STAMP(11, 12);
  const u32 ofo = (w >> 1) * 20 * 32768 + kq * 32768 + (tokA + (w & 1) * 16 + l15) * 16;
#pragma unroll
  for (int ks = 0; ks < 5; ++ks) ld128_sc1(of[ks], ws, ofo, WS_O + ks * 4 * 32768);
  PIN(g0); PIN(g1); PIN(xv0); PIN(xv1);
  spin_gran(g0, ws, go, 0, tag); spin_gran(g1, ws, go, 8192, tag);
  const float ga = __uint_as_float(g0[0]), gb = __uint_as_float(g1[0]);
  float ba[8], bb[8];
#pragma unroll
  for (int e = 0; e < 4; ++e) {
    ba[2 * e] = (1.f - ga) * blo(xv0[e]); ba[2 * e + 1] = (1.f - ga) * bhi(xv0[e]);
    bb[2 * e] = (1.f - gb) * blo(xv1[e]); bb[2 * e + 1] = (1.f - gb) * bhi(xv1[e]);
  }
  u32 ared = w * 5440 + (lane >> 5) * 544 + (lane & 31) * 16, afin = (tk >> 4) * 5440 + (pc >> 1) * 1088 + (pc & 1) * 544 + (tk & 15) * 16;
  u32 ared1 = ared + 43520, afin1 = afin + 43520;
  PIN(ared); PIN(afin); PIN(ared1); PIN(afin1);
  rsrc_t rO = mk_rsrc(p.out, 2048 * 1280);
  BAR();
  WAITV(0);
#pragma unroll
  for (int ks = 0; ks < 5; ++ks) PIN(of[ks]);
  ld32_sc1(flv, ws, fa, 256 * 128);
  o_compute(s, ared, bw, of);
  STAMP(12, 13);
  for (u32 m = (u32)__ballot(lane < 8 && ((seltok & 1023u) - (u32)tokA) < 32u); m; m &= m - 1) {
    const int c = __builtin_ctz(m);
    const u32 key = __builtin_amdgcn_readlane(selkey, c), tok = 2047 - (key & 0xffffu);
    const int sp = (tid * 3277) >> 16, c4 = tid - sp * 20;
    const u32 yo = WS_Y + (((sp < 24 ? sp : 0) * 8 + c) * 160 + sl * 20 + c4) * 16;
    u32x4 y;
#pragma unroll 1
    for (int it = 0; it < SPIN_MAX; ++it) { ld128_sc1(y, ws, yo, 0); WAITV(0); PIN(y); if (__all(y[3] == tag)) break; }
    *(f32x4*)(s + L_A1 + tid * 16) = gran_unpack(y);
    BAR();
    if (tid < 80) { float t = 0.f;
#pragma unroll
      for (int q = 0; q < 24; ++q) t += *(float*)(s + L_A1 + q * 320 + tid * 4);
      *(float*)(s + L_YS + tid * 4) = t; }
    BAR();
    if ((u32)(tokA + tk) == (tok & 1023u)) {
      const u32 o = key >> 16, rb = (o & 0x8000u) ? (o ^ 0x8000u) : (o ^ 0xffffu);
      const float rs = sigmoidf_(__uint_as_float(rb << 16));
      const f32x4 ya = *(f32x4*)(s + L_YS + pc * 32), yb = *(f32x4*)(s + L_YS + pc * 32 + 16);
      const float fa_ = tok < 1024 ? (1.f - ga) * rs : 0.f, fb_ = tok < 1024 ? 0.f : (1.f - gb) * rs;
#pragma unroll
      for (int e = 0; e < 4; ++e) { ba[e] += fa_ * ya[e]; ba[4 + e] += fa_ * yb[e]; bb[e] += fb_ * ya[e]; bb[4 + e] += fb_ * yb[e]; }
    }
  }
  BAR();
  if (b == 0 && w == 7) {
    u32 f[4];
#pragma unroll
    for (int i = 0; i < 4; ++i) ld32_sc1(f[i], ws, (lane & 15) < 15 ? WS_FG + (i * 64 + lane) * 128 : WS_CAND + (i * 4 + kq) * 128 + 4, 0);
    WAITV(0);
#pragma unroll
    for (int i = 0; i < 4; ++i) { PIN(f[i]); spin_flags(f[i], ws, (lane & 15) < 15 ? WS_FG + (i * 64 + lane) * 128 : WS_CAND + (i * 4 + kq) * 128 + 4, 0, tag); }
    if (lane == 0) st32_sc1(tag, ws, WS_EP);
  }
  WAITV(0); PIN(flv);
  const bool rdy = __all(flv == tag);
  if (!rdy) { o_final(s, afin, ga, ba, rO, xo, 0); spin_flags(flv, ws, fa, 256 * 128, tag); }
  STAMP(13, 14);
#pragma unroll
  for (int ks = 0; ks < 5; ++ks) ld128_sc1(of[ks], ws, ofo, WS_O + ks * 4 * 32768 + 1024 * 16);
  if (rdy) { o_final(s, afin, ga, ba, rO, xo, 0); WAITV(1); } else WAITV(0);
#pragma unroll
  for (int ks = 0; ks < 5; ++ks) PIN(of[ks]);
  STAMP(14, 15);
  o_compute(s, ared1, bw, of);
  BAR();
  STAMP(15, 16);
  if (w < 5) o_final(s, afin1, gb, bb, rO, xo, 1024 * 1280);
  STAMP(16, 17);
  asm volatile("fk_code_hi:" ::: "memory");
}

extern "C" void kernel_launch(void* const* d_in, const int* in_sizes, int n_in, void* d_out, int out_size, void* d_ws, size_t ws_size,
                              hipStream_t stream) {
  Params p;
  p.x = (const u16*)d_in[0]; p.wqkv = (const u16*)d_in[1]; p.wo = (const u16*)d_in[2]; p.wr = (const u16*)d_in[4];
  p.wg = (const u16*)d_in[5]; p.wu = (const u16*)d_in[6]; p.wd = (const u16*)d_in[7]; p.gw = (const u16*)d_in[8];
  p.out = (u16*)d_out; p.ws = (char*)d_ws;
  (void)hipFuncSetAttribute((const void*)fused_kernel, hipFuncAttributeMaxDynamicSharedMemorySize, LDS_BYTES);
  fused_kernel<<<256, 512, LDS_BYTES, stream>>>(p);
}
```

```cpp
#define STAMPS
#include <hip/hip_runtime.h>
#include <hip/hip_bf16.h>

typedef unsigned short u16;
typedef unsigned int u32;
typedef unsigned long long u64;
typedef __attribute__((ext_vector_type(2))) u32 u32x2;
typedef __attribute__((ext_vector_type(4))) u32 u32x4;
typedef __attribute__((ext_vector_type(4))) float f32x4;
typedef __attribute__((ext_vector_type(8))) __bf16 bf16x8;
typedef __attribute__((ext_vector_type(2))) __bf16 bf16x2;
typedef __attribute__((ext_vector_type(4))) short s16x4;
#define LDSP(T) __attribute__((address_space(3))) T*
typedef __amdgpu_buffer_rsrc_t rsrc_t;
#define DEV __device__ __forceinline__

constexpr u32 WS_QKV = 0;
constexpr u32 WS_O   = WS_QKV + 240u * 32768;
constexpr u32 WS_P1  = WS_O + 80u * 32768;
constexpr u32 WS_Y   = WS_P1 + 480u * 1024;
constexpr u32 WS_FG  = WS_Y + 24u * 8 * 160 * 16;
constexpr u32 WS_FA  = WS_FG + 512u * 128;
constexpr u32 WS_CAND = WS_FA + 512u * 128;
constexpr u32 WS_GG  = WS_CAND + 16u * 128;
constexpr u32 WS_EP  = WS_GG + 2048u * 8;
constexpr u32 WS_END = WS_EP + 128;
constexpr u32 WS_STAMP = WS_END;

constexpr u32 L_X1   = 73728;
constexpr u32 L_F1W  = 51200;
constexpr u32 L_WOB  = 61440;
constexpr u32 L_A0   = 76800;
constexpr u32 L_A1   = 114048;
constexpr u32 L_F2W  = 151296;
constexpr u32 L_RW   = 155648;
constexpr u32 L_CNT  = 159488;
constexpr u32 L_SEL  = 159552;
constexpr u32 L_H    = 159744;
constexpr u32 L_KEYS = 160768;
constexpr u32 L_RB   = 161280;
constexpr u32 L_YS   = 161280;
constexpr u32 LDS_BYTES = 163840;
constexpr int SPIN_MAX = 1 << 14;
constexpr u32 OOB = 0x80000000u;

struct Params {
  const u16* x; const u16* wqkv; const u16* wo; const u16* wr; const u16* wg; const u16* wu; const u16* wd; const u16* gw;
  u16* out; char* ws;
};

constexpr int NSTAMP = 31;
struct Stamps { u32 v; };
template <int L> __device__ __forceinline__ void stamp_(u32& v) { asm volatile("v_writelane_b32 %0, s100, %1\n\ts_memrealtime s[100:101]" : "+v"(v) : "n"(L) : "s100", "s101"); }
#define STAMP(prev, i) stamp_<prev>(st.v)
#define STAMP_ARG , Stamps& st
#define STAMP_PASS , st

DEV rsrc_t mk_rsrc(const void* p, u32 bytes) { return __builtin_amdgcn_make_buffer_rsrc((void*)p, 0, bytes, 0x00020000); }
DEV u32 rfl(u32 v) { return (u32)__builtin_amdgcn_readfirstlane((int)v); }
template <u32 L> DEV void dma(rsrc_t r, u32 voff, u32 soff, u32 lds) {
  asm volatile("s_add_u32 m0, %0, %4\n\ts_nop 0\n\tbuffer_load_dwordx4 %1, %2, %3 offen lds" :: "s"(lds), "v"(voff), "s"(r), "s"(soff), "n"(L) : "memory", "scc");
}
template <u32 L> DEV void dma_sc1(rsrc_t r, u32 voff, u32 soff, u32 lds) {
  asm volatile("s_add_u32 m0, %0, %4\n\ts_nop 0\n\tbuffer_load_dwordx4 %1, %2, %3 offen sc1 lds" :: "s"(lds), "v"(voff), "s"(r), "s"(soff), "n"(L) : "memory", "scc");
}
template <u32 L> DEV void dma3(rsrc_t rX, rsrc_t rW, u32 vX, u32 vW0, u32 vW1, u32 sX, u32 sW, u32 lds) {
  asm volatile("s_add_u32 m0, %0, %8\n\ts_nop 0\n\tbuffer_load_dwordx4 %1, %2, %3 offen lds\n\t"
               "buffer_load_dwordx4 %4, %6, %7 offen offset:1024 lds\n\tbuffer_load_dwordx4 %5, %6, %7 offen offset:2048 lds"
               :: "s"(lds), "v"(vX), "s"(rX), "s"(sX), "v"(vW0), "v"(vW1), "s"(rW), "s"(sW), "n"(L) : "memory", "scc");
}
template <u32 I> DEV void dma_cont(rsrc_t r, u32 voff, u32 soff) {
  asm volatile("buffer_load_dwordx4 %0, %1, %2 offen offset:%3 lds" :: "v"(voff), "s"(r), "s"(soff), "n"(I) : "memory");
}
template <u32 I = 0> DEV void ld128_sc1(u32x4& d, rsrc_t r, u32 voff, u32 soff) { asm volatile("buffer_load_dwordx4 %0, %1, %2, %3 offen offset:%4 sc1" : "=&v"(d) : "v"(voff), "s"(r), "s"(soff), "n"(I) : "memory"); }
template <u32 I = 0> DEV void ld128(u32x4& d, rsrc_t r, u32 voff, u32 soff) { asm volatile("buffer_load_dwordx4 %0, %1, %2, %3 offen offset:%4" : "=&v"(d) : "v"(voff), "s"(r), "s"(soff), "n"(I) : "memory"); }
template <u32 I> DEV void gld128(u32x4& d, const char* a) { asm volatile("global_load_dwordx4 %0, %1, off offset:%2" : "=&v"(d) : "v"(a), "n"(I) : "memory"); }
DEV void ld64_sc1(u32x2& d, rsrc_t r, u32 voff, u32 soff) { asm volatile("buffer_load_dwordx2 %0, %1, %2, %3 offen sc1" : "=&v"(d) : "v"(voff), "s"(r), "s"(soff) : "memory"); }
DEV void ld32_sc1(u32& d, rsrc_t r, u32 voff, u32 soff) { asm volatile("buffer_load_dword %0, %1, %2, %3 offen sc1" : "=&v"(d) : "v"(voff), "s"(r), "s"(soff) : "memory"); }
#define WAITV(n) asm volatile("s_waitcnt vmcnt(%0)" :: "n"(n) : "memory")
template <class T> DEV void pin_(T& x) { asm volatile("" : "+v"(x)); }
#define PIN(x) pin_(x)
#define BAR() do { asm volatile("" ::: "memory"); __builtin_amdgcn_s_waitcnt(0xC07F); asm volatile("s_barrier" ::: "memory"); } while (0)
#define SCHED() __builtin_amdgcn_sched_barrier(0)
DEV void st64_sc1(u32x2 v, rsrc_t r, u32 voff, u32 soff = 0) { __builtin_amdgcn_raw_buffer_store_b64(v, r, voff, soff, 16); }
DEV void st128_sc1(u32x4 v, rsrc_t r, u32 voff) { __builtin_amdgcn_raw_buffer_store_b128(v, r, voff, 0, 16); }
DEV void st32_sc1(u32 v, rsrc_t r, u32 voff) { __builtin_amdgcn_raw_buffer_store_b32(v, r, voff, 0, 16); }

DEV u32 pk(float a, float b) { bf16x2 v = {(__bf16)a, (__bf16)b}; return __builtin_bit_cast(u32, v); }
DEV float rbf(float a) { return (float)(__bf16)a; }
DEV float blo(u32 v) { return __uint_as_float(v << 16); }
DEV float bhi(u32 v) { return __uint_as_float(v & 0xffff0000u); }
DEV f32x4 mfma(u32x4 a, u32x4 b, f32x4 c) { return __builtin_amdgcn_mfma_f32_16x16x32_bf16(__builtin_bit_cast(bf16x8, a), __builtin_bit_cast(bf16x8, b), c, 0, 0, 0); }
DEV u32x2 trrd(char* s, u32 a) { return __builtin_bit_cast(u32x2, __builtin_amdgcn_ds_read_tr16_b64_v4i16((LDSP(s16x4))(s + a))); }
DEV u32x4 lds128(char* s, u32 a) { return *(u32x4*)(s + a); }
DEV u32x4 cat(u32x2 a, u32x2 b) { u32x4 r = {a[0], a[1], b[0], b[1]}; return r; }
DEV float ex2(float v) { return __builtin_amdgcn_exp2f(v); }
DEV float sigmoidf_(float v) { return __builtin_amdgcn_rcpf(1.f + ex2(v * -1.4426950408889634f)); }
DEV u32x4 gran_pack(f32x4 c, u32 tag) {
  const u32 a = __float_as_uint(c[0]), b = __float_as_uint(c[1]), d = __float_as_uint(c[2]), e = __float_as_uint(c[3]);
  u32x4 r = {__builtin_amdgcn_perm(a, e, 0x07060503u), __builtin_amdgcn_perm(b, e, 0x07060502u), __builtin_amdgcn_perm(d, e, 0x07060501u), tag};
  return r;
}
DEV f32x4 gran_unpack(u32x4 g) {
  const u32 t = __builtin_amdgcn_perm(g[0], g[1], 0x04000c0cu);
  f32x4 r = {__uint_as_float(g[0]), __uint_as_float(g[1]), __uint_as_float(g[2]), __uint_as_float(__builtin_amdgcn_perm(t, g[2], 0x0706000cu))};
  return r;
}

DEV void arrive_flag(char* s, int cnt, rsrc_t ws, u32 flagoff, u32 tag, int lane) {
  if (lane == 0) {
    u32 old;
    asm volatile("ds_add_rtn_u32 %0, %1, %2\n\ts_waitcnt lgkmcnt(0)" : "=v"(old) : "v"((u32)(size_t)(LDSP(char))(s + L_CNT + cnt * 4)), "v"(1u) : "memory");
    st32_sc1(tag, ws, old == 7 ? flagoff : OOB);
  }
}
DEV void spin_flags(u32 v, rsrc_t ws, u32 voff, u32 soff, u32 tag) {
#pragma unroll 1
  for (int i = 0; i < SPIN_MAX && !__all(v == tag); ++i) { ld32_sc1(v, ws, voff, soff); WAITV(0); PIN(v); }
}
DEV void spin_gran(u32x2& v, rsrc_t ws, u32 voff, u32 soff, u32 tag) {
#pragma unroll 1
  for (int i = 0; i < SPIN_MAX && !__all(v[1] == tag); ++i) { ld64_sc1(v, ws, voff, soff); WAITV(0); PIN(v); }
}
DEV u32 rank128(char* s, int tid, u32& key) {
  key = *(u32*)(s + L_KEYS + (tid >> 2) * 4);
  u32 c = 0;
#pragma unroll
  for (int i = 0; i < 8; ++i) {
    u32x4 k = lds128(s, L_KEYS + (tid & 3) * 128 + i * 16);
    c += (k[0] > key) + (k[1] > key) + (k[2] > key) + (k[3] > key);
  }
  c += __builtin_amdgcn_update_dpp(0u, c, 0xB1, 0xf, 0xf, true);
  c += __builtin_amdgcn_update_dpp(0u, c, 0x4E, 0xf, 0xf, true);
  return c;
}
DEV void f1w_stage(const Params& p, u32 sb, int w, int lane, int xcd, int loc) {
  const int s32 = xcd * 6 + (loc < 30 ? loc / 5 : 0), kr = loc % 5;
  const u32 vo = (16 * w + (lane >> 2)) * 3072 + (((lane & 3) ^ ((lane >> 5) * 2)) * 16), so = kr * (128 * 3072) + s32 * 64;
  dma<L_F1W>(mk_rsrc(p.wg, 640 * 1536 * 2), vo, so, sb + w * 1024);
  dma<L_F1W + 8192>(mk_rsrc(p.wu, 640 * 1536 * 2), vo, so, sb + w * 1024);
}

DEV void g_epilogue(f32x4 (&acc)[2][2], rsrc_t ws, u32 voff, u32 soff, bool isq) {
  if (isq) {
#pragma unroll
    for (int i = 0; i < 4; ++i) acc[i >> 1][i & 1] *= 0.32259863752095353f;
  }
#pragma unroll
  for (int ft = 0; ft < 2; ++ft)
#pragma unroll
    for (int tt = 0; tt < 2; ++tt) {
      f32x4 a = acc[ft][tt];
      u32x2 v = {pk(a[0], a[1]), pk(a[2], a[3])};
      st64_sc1(v, ws, voff + tt * 256, soff + ft * 65536);
    }
}
constexpr u32 ring_slot(int i) { return (i & 3) == 3 ? L_X1 + 7 * 8192 : (i & 3) * 24576; }
template <int I> struct IC { static constexpr int v = I; };
template <int N, class F> DEV void static_for(F&& f) { if constexpr (N > 0) { static_for<N - 1>(f); f(IC<N - 1>{}); } }

DEV void g_phase(const Params& p, char* s, u32 sb, rsrc_t ws, int w, int lane, int r, int ct, int rot, int xcd, int loc,
                 u32& tag, u32 epv, u32 pf, u32x2& cand, u32 candoff STAMP_ARG) {
  const int cg = w & 3, th = w >> 2, kq = lane >> 4, l15 = lane & 15;
  rsrc_t rW = mk_rsrc((const char*)p.wqkv - 2048, 640 * 3840 + 2048), rX = mk_rsrc(p.x, 2048 * 1280);
  const u32 vW = kq * 3840 + ((l15 ^ (2 * (kq | ((w & 1) << 2)))) * 16),
    vW0 = vW + 1024, vW1 = vW + 4 * 3840;
  const u32 vX = (lane >> 3) * 1280 + (((lane & 7) ^ ((4 * w + kq) & 7)) * 16);
  const u32 sW = 8 * w * 3840 + ct * 256, sX = (64 * r + 8 * w) * 1280, sb3 = sb + w * 3072, sb1 = sb + w * 1024;
  u32 k0 = rot * 128, k1 = rot * 128;
  auto batch = [&](auto ic) { constexpr int i = decltype(ic)::v;
    dma3<ring_slot(i)>(rX, rW, vX, vW0, vW1, sX + k0, sW + k0 * 1920, sb3); k0 = k0 == 1152 ? 0 : k0 + 128; };
  auto x1 = [&](auto jc) { constexpr int j = decltype(jc)::v;
    dma<L_X1 + j * 8192>(rX, vX, sX + k1 + 1024 * 1280, sb1); k1 = k1 == 1152 ? 0 : k1 + 128; };
  STAMP(0, 18);
  batch(IC<0>{});
  asm volatile("s_barrier" ::: "memory");
  batch(IC<1>{});
  asm volatile("s_barrier" ::: "memory");
  batch(IC<2>{});
  STAMP(18, 19);
  const int qp = (lane >> 2) & 3, pp = lane & 3, hrow = qp | ((kq & 1) << 2);
  u32 aW0 = kq * 3072 + 1024 + qp * 256 + (((cg * 4 + (pp >> 1)) ^ (2 * hrow)) * 16) + (pp & 1) * 8, aW1 = aW0 ^ 32;
  const u32 xsw = (kq ^ (l15 >> 1)) * 16;
  u32 aR0 = (4 * th + (l15 >> 3)) * 3072 + (l15 & 7) * 128 + xsw, aR1 = aR0 ^ 64;
  u32 aS0 = L_X1 + (32 * th + l15) * 128 + xsw, aS1 = aS0 ^ 64;
  PIN(aW0); PIN(aW1); PIN(aR0); PIN(aR1); PIN(aS0); PIN(aS1);
  u32x4 wf[20][2];
  f32x4 acc[2][2] = {}, acd[2][2] = {};
  u32x4 xf[2][4], xh[10][4];
  auto mm0 = [&](auto ic, int h) { constexpr int i = decltype(ic)::v;
#pragma unroll
    for (int q = 0; q < 4; ++q) acc[q & 1][q >> 1] = mfma(wf[2 * i + h][q & 1], xf[i & 1][2 * h + (q >> 1)], acc[q & 1][q >> 1]); };
  auto rd1 = [&](int j) {
#pragma unroll
    for (int q = 0; q < 4; ++q) xh[j][q] = lds128(s, ((q >> 1) ? aS1 : aS0) + (j < 6 ? j : j - 6) * 8192 + (q & 1) * 2048); };
  auto mm1 = [&](int j) {
#pragma unroll
    for (int q = 0; q < 8; ++q) acd[q & 1][q >> 1 & 1] = mfma(wf[2 * j + (q >> 2)][q & 1], xh[j][q >> 1], acd[q & 1][q >> 1 & 1]); };
  static_for<10>([&](auto ic) { constexpr int i = decltype(ic)::v;
    constexpr u32 base = (i & 3) == 3 ? 0 : ring_slot(i), jump = (i & 3) == 3 ? ring_slot(3) : (i & 3) == 0 && i ? -ring_slot(3) : 0;
    if constexpr (jump != 0) { aW0 += jump; aW1 += jump; aR0 += jump; aR1 += jump; PIN(aW0); PIN(aW1); PIN(aR0); PIN(aR1); }
    const u32 kx = sX + k0, kw = sW + k0 * 1920;
    k0 = k0 == 1152 ? 0 : k0 + 128;
    WAITV(i < 8 ? 6 : i == 8 ? 5 : 4);
    BAR();
    if constexpr (i < 7) dma<ring_slot(i + 3)>(rX, vX, kx, sb3); else x1(IC<2 * i - 14>{});
    if (i == 0) { PIN(epv); PIN(pf); tag = rfl(epv) * 1664525u + 1013904223u; STAMP(19, 20); }
#pragma unroll
    for (int ks = 0; ks < 2; ++ks) {
      wf[2 * i + ks][0] = cat(trrd(s, base + aW0 + ks * 12288), trrd(s, base + aW0 + ks * 12288 + 1024));
      xf[i & 1][2 * ks] = lds128(s, base + (ks ? aR1 : aR0));
      wf[2 * i + ks][1] = cat(trrd(s, base + aW1 + ks * 12288), trrd(s, base + aW1 + ks * 12288 + 1024));
      xf[i & 1][2 * ks + 1] = lds128(s, base + (ks ? aR1 : aR0) + 6144);
    }
    SCHED();
    if constexpr (i > 0) mm0(IC<i - 1>{}, 0);
    SCHED();
    if constexpr (i < 7) dma_cont<1024>(rW, vW0, kw);
    SCHED();
    if constexpr (i > 0) mm0(IC<i - 1>{}, 1);
    SCHED();
    if constexpr (i < 7) dma_cont<2048>(rW, vW1, kw); else x1(IC<2 * i - 13>{});
  });
  mm0(IC<9>{}, 0); mm0(IC<9>{}, 1);
  STAMP(20, 1);
  const u32 vst = (kq >> 1) * 32768 + l15 * 16 + (kq & 1) * 8, sst = WS_QKV + (ct * 16 + cg * 4) * 32768 + (64 * r + 32 * th) * 16;
  g_epilogue(acc, ws, vst, sst, ct < 5);
  WAITV(4);
  BAR();
  STAMP(1, 2);
  rd1(0); rd1(1); SCHED();
  x1(IC<6>{}); rd1(2); mm1(0); SCHED();
  x1(IC<7>{}); rd1(3); mm1(1); SCHED();
  x1(IC<8>{}); rd1(4); mm1(2); SCHED();
  x1(IC<9>{}); rd1(5); mm1(3); SCHED();
  aS0 += 6 * 8192; aS1 += 6 * 8192; PIN(aS0); PIN(aS1);
  mm1(4); SCHED();
  WAITV(0);
  arrive_flag(s, 0, ws, WS_FG + (r * 16 + ct) * 128, tag, lane);
  BAR();
  STAMP(2, 21);
  rd1(6); rd1(7);
  if (w < 2) ld64_sc1(cand, ws, candoff, 0);
  mm1(5); SCHED();
  f1w_stage(p, sb, w, lane, xcd, loc);
  rd1(8); mm1(6); SCHED();
  rd1(9); mm1(7); SCHED();
  mm1(8); mm1(9);
  STAMP(21, 3);
  g_epilogue(acd, ws, vst, sst + 1024 * 16, ct < 5);
  WAITV(4);
}

DEV void r_phase(const Params& p, char* s, u32 sb, rsrc_t ws, int w, int lane, int tid, int r, int xcd, int loc,
                 u32& tag, u32 epv, u32 pf, u32x2& cand, u32 candoff STAMP_ARG) {
  const int kq = lane >> 4, l15 = lane & 15;
  rsrc_t rX = mk_rsrc(p.x, 2048 * 1280);
  const u32 vX = (lane >> 3) * 1280 + (((lane & 7) ^ ((4 * w + kq) & 7)) * 16), sX = (64 * r + 8 * w) * 1280, sb1 = sb + w * 1024;
  static_for<9>([&](auto ic) { constexpr int i = decltype(ic)::v; dma<i * 8192>(rX, vX, sX + i * 128, sb1); });
  static_for<10>([&](auto ic) { constexpr int i = decltype(ic)::v; dma<L_X1 + i * 8192>(rX, vX, sX + i * 128 + 1024 * 1280, sb1); });
  if ((w & 1) * 1024 + lane * 16 < 1280) {
    if (w < 2) dma<L_RW>(mk_rsrc(p.wr, 1280), w * 1024 + lane * 16, 0, sb1);
    else if (w < 4) dma<L_RW + 1536 - 2048>(mk_rsrc(p.gw, 1280), (w - 2) * 1024 + lane * 16, 0, sb1);
  }
  u32x4 xa, xb;
  ld128<1152>(xa, rX, (64 * r + 16 * (w & 3) + l15) * 1280 + kq * 16, 0); ld128<1216>(xb, rX, (64 * r + 16 * (w & 3) + l15) * 1280 + kq * 16, 0);
  u32 aw = L_RW + (l15 == 1 ? 1536 : 0) + kq * 16;
  u32 aX0 = ((w >> 2) ? L_X1 : 0) + (16 * (w & 3) + l15) * 128 + ((kq ^ (l15 >> 1)) * 16), aX1 = aX0 ^ 64, aX2 = aX0 + 65536, aX3 = aX1 + 65536;
  PIN(aw); PIN(aX0); PIN(aX1); PIN(aX2); PIN(aX3);
  f32x4 acc = {0.f, 0.f, 0.f, 0.f};
  WAITV(0); BAR();
  STAMP(0, 29);
  PIN(epv); PIN(pf); PIN(xa); PIN(xb); tag = rfl(epv) * 1664525u + 1013904223u;
  if (w >> 2) { xa = lds128(s, 8192 + aX2); xb = lds128(s, 8192 + aX3); }
  u32x4 wf[20], xr[18];
#pragma unroll
  for (int i = 0; i < 9; ++i) {
    wf[2 * i] = lds128(s, aw + i * 128); xr[2 * i] = lds128(s, i < 8 ? i * 8192 + aX0 : aX2);
    wf[2 * i + 1] = lds128(s, aw + i * 128 + 64); xr[2 * i + 1] = lds128(s, i < 8 ? i * 8192 + aX1 : aX3);
  }
  wf[18] = lds128(s, aw + 1152); wf[19] = lds128(s, aw + 1216);
  SCHED();
#pragma unroll
  for (int i = 0; i < 18; ++i) acc = mfma(xr[i], wf[i], acc);
  acc = mfma(xa, wf[18], acc);
  acc = mfma(xb, wf[19], acc);
  if (l15 < 2) *(f32x4*)(s + L_RB + l15 * 512 + (16 * w + 4 * kq) * 4) = acc;
  BAR();
  STAMP(29, 30);
  u32x2 gr;
  if (tid < 128) {
    const int tok = (tid >> 6) * 1024 + 64 * r + (tid & 63);
    const float rv = *(float*)(s + L_RB + tid * 4), gl = *(float*)(s + L_RB + 512 + tid * 4);
    const u32 rb = pk(rv, 0.f) & 0xffffu;
    *(u32*)(s + L_KEYS + tid * 4) = ((rb ^ ((rb & 0x8000u) ? 0xffffu : 0x8000u)) << 16) | (u32)(2047 - tok);
    gr[0] = __float_as_uint(sigmoidf_(gl)); gr[1] = tag;
  }
  BAR();
  u32 key; const u32 rk = rank128(s, tid, key);
  if (rk < 8 && (tid & 3) == 0) { u32x2 c = {key, tag}; st64_sc1(c, ws, WS_CAND + r * 128 + rk * 8); }
  if (tid < 128) st64_sc1(gr, ws, WS_GG + ((tid >> 6) * 1024 + 64 * r + (tid & 63)) * 8);
  f1w_stage(p, sb, w, lane, xcd, loc);
  STAMP(30, 3);
  BAR();
  if (w < 2) ld64_sc1(cand, ws, candoff, 0);
  WAITV(0);
}

template <u32 BUF> DEV void attn_stage(rsrc_t ws, u32 sb1, u32 so, int lane) {
  dma_sc1<BUF>(ws, lane * 16, so, sb1);
  dma_sc1<BUF + 6144>(ws, lane * 16, so + 32768, sb1);
  dma_sc1<BUF + 12288>(ws, lane * 16, so + 65536, sb1);
  dma_sc1<BUF + 18432>(ws, lane * 16, so + 80 * 32768, sb1);
  dma_sc1<BUF + 18432 + 6272>(ws, lane * 16, so + 81 * 32768, sb1);
  dma_sc1<BUF + 18432 + 12544>(ws, lane * 16, so + 82 * 32768, sb1);
}
DEV float xmax(float m) {
  auto a = __builtin_amdgcn_permlane16_swap(__float_as_uint(m), __float_as_uint(m), false, false);
  m = fmaxf(__uint_as_float(a[0]), __uint_as_float(a[1]));
  auto b = __builtin_amdgcn_permlane32_swap(__float_as_uint(m), __float_as_uint(m), false, false);
  return fmaxf(__uint_as_float(b[0]), __uint_as_float(b[1]));
}
template <class Mid>
DEV void attn_unit(char* s, u32x4 qf, u32 aK, u32 aV0, u32 aV1, int nskip, int lane, rsrc_t ws, u32 vo0, u32 vo1, u32 so, Mid&& mid) {
  const int kq = lane >> 4, l15 = lane & 15, dg = l15 - 4 * kq;
  const float NEG = -__builtin_inff();
  const f32x4 z = {0.f, 0.f, 0.f, 0.f};
  const u32x2 zz = {0u, 0u};
  u32x4 kf[18], va[9], vb[9];
  kf[16] = lds128(s, aK + 16 * 256); kf[0] = lds128(s, aK); kf[1] = lds128(s, aK + 256);
  va[8] = cat(trrd(s, aV0 + 16 * 256), zz); vb[8] = cat(trrd(s, aV1 + 16 * 256), zz);
  kf[2] = lds128(s, aK + 512); kf[3] = lds128(s, aK + 768);
  va[0] = cat(trrd(s, aV0), trrd(s, aV0 + 256)); vb[0] = cat(trrd(s, aV1), trrd(s, aV1 + 256));
  SCHED();
  f32x4 sd = mfma(kf[16], qf, z);
#pragma unroll
  for (int e = 0; e < 4; ++e) sd[e] = e <= dg ? sd[e] : NEG;
  const float m = xmax(fmaxf(fmaxf(sd[0], sd[1]), fmaxf(sd[2], sd[3])));
  const f32x4 c = {-m, -m, -m, -m};
  f32x4 sa = mfma(kf[0], qf, c), sc = mfma(kf[1], qf, c);
  u32x4 P = {pk(ex2(sd[0] - m), ex2(sd[1] - m)), pk(ex2(sd[2] - m), ex2(sd[3] - m)), 0u, 0u};
  f32x4 o0 = mfma(va[8], P, z), o1 = mfma(vb[8], P, z);
#pragma unroll
  for (int e = 0; e < 4; ++e) sa[e] = e > dg ? sa[e] : NEG;
  static_for<8>([&](auto bc) { constexpr int b = decltype(bc)::v;
    SCHED();
    f32x4 na, nc;
    if (b < 7) na = mfma(kf[2 * b + 2], qf, c);
    if (b < 6) { kf[2 * b + 4] = lds128(s, aK + (2 * b + 4) * 256); kf[2 * b + 5] = lds128(s, aK + (2 * b + 5) * 256); }
    P[0] = pk(ex2(sa[0]), ex2(sa[1])); P[1] = pk(ex2(sa[2]), ex2(sa[3]));
    SCHED();
    if (b < 7) nc = mfma(kf[2 * b + 3], qf, c);
    if (b < 7) {
      va[b + 1] = cat(trrd(s, aV0 + (2 * b + 2) * 256), trrd(s, aV0 + (2 * b + 3) * 256));
      vb[b + 1] = cat(trrd(s, aV1 + (2 * b + 2) * 256), trrd(s, aV1 + (2 * b + 3) * 256));
    }
    P[2] = pk(ex2(sc[0]), ex2(sc[1])); P[3] = pk(ex2(sc[2]), ex2(sc[3]));
    SCHED();
    o0 = mfma(va[b], P, o0);
    o1 = mfma(vb[b], P, o1);
    mid(bc);
    sa = na; sc = nc;
  });
  SCHED();
  auto sw = __builtin_amdgcn_permlane16_swap(__float_as_uint(o1[0]), __float_as_uint(o1[0]), false, false);
  float l = kq == 0 ? __uint_as_float(sw[1]) : o1[0];
  {
    const float big = fmaxf(fmaxf(fmaxf(fabsf(o0[0]), fabsf(o0[1])), fmaxf(fabsf(o0[2]), fabsf(o0[3]))),
                            fmaxf(fmaxf(fabsf(o1[0]), fabsf(o1[1])), fmaxf(fabsf(o1[2]), fabsf(o1[3]))));
    const float sum = (o0[0] + o0[1]) + (o0[2] + o0[3]) + (o1[0] + o1[1]) + (o1[2] + o1[3]);
    const bool ok = l < 1.8446744073709552e19f && big < 1e37f && sum == sum;
    if (__builtin_expect(!__all(ok), 0)) {
      float mt = m;
#pragma unroll 1
      for (int t = nskip; t < 16; ++t) {
        f32x4 v = mfma(lds128(s, aK + t * 256), qf, z);
#pragma unroll
        for (int e = 0; e < 4; ++e) mt = fmaxf(mt, (t > 0 || e > dg) ? v[e] : NEG);
      }
      mt = xmax(mt);
      o0 = z; o1 = z;
#pragma unroll 1
      for (int t = nskip; t < 17; ++t) {
        f32x4 v = mfma(lds128(s, aK + t * 256), qf, z);
#pragma unroll
        for (int e = 0; e < 4; ++e) v[e] = (t == 0 ? e > dg : (t < 16 || e <= dg)) ? ex2(v[e] - mt) : 0.f;
        const u32x4 Q = {pk(v[0], v[1]), pk(v[2], v[3]), 0u, 0u};
        o0 = mfma(cat(trrd(s, aV0 + t * 256), zz), Q, o0);
        o1 = mfma(cat(trrd(s, aV1 + t * 256), zz), Q, o1);
      }
      auto s2 = __builtin_amdgcn_permlane16_swap(__float_as_uint(o1[0]), __float_as_uint(o1[0]), false, false);
      l = kq == 0 ? __uint_as_float(s2[1]) : o1[0];
    }
  }
  const float inv = __builtin_amdgcn_rcpf(l);
  u32x2 v0 = {pk(o0[0] * inv, o0[1] * inv), pk(o0[2] * inv, o0[3] * inv)};
  u32x2 v1 = {pk(o1[0] * inv, o1[1] * inv), pk(o1[2] * inv, o1[3] * inv)};
  st64_sc1(v0, ws, vo0, so);
  st64_sc1(v1, ws, vo1, so);
}

DEV void o_compute(char* s, u32 a, u32x4 (&bw)[5][5], u32x4 (&of)[5]) {
  f32x4 acc[5] = {};
#pragma unroll
  for (int ks = 0; ks < 5; ++ks)
#pragma unroll
    for (int nt = 0; nt < 5; ++nt) acc[nt] = mfma(bw[ks][nt], of[ks], acc[nt]);
#pragma unroll
  for (int nt = 0; nt < 5; ++nt) *(f32x4*)(s + a + nt * 1088) = acc[nt];
}
DEV void o_final(char* s, u32 a, float g, const float (&b)[8], rsrc_t rO, u32 vo, u32 so) {
  f32x4 lo = *(f32x4*)(s + a), hi = *(f32x4*)(s + a + 256);
#pragma unroll
  for (int q = 1; q < 4; ++q) { lo += *(f32x4*)(s + a + q * 10880); hi += *(f32x4*)(s + a + q * 10880 + 256); }
  u32x4 o = {pk(g * lo[0] + b[0], g * lo[1] + b[1]), pk(g * lo[2] + b[2], g * lo[3] + b[3]),
             pk(g * hi[0] + b[4], g * hi[1] + b[5]), pk(g * hi[2] + b[6], g * hi[3] + b[7])};
  __builtin_amdgcn_raw_buffer_store_b128(o, rO, vo, so, 0);
}

__global__ __launch_bounds__(512, 2) void fused_kernel(Params p) {
  extern __shared__ __attribute__((aligned(16))) char smem[];
  char* s = smem;
  const int tid = threadIdx.x, lane = tid & 63;
  Stamps st; st.v = 0; asm volatile("s_memrealtime s[100:101]" ::: "s100", "s101");
  u32 pf;
  {
    u64 pc; u32 len;
    asm volatile("s_getpc_b64 %0\nfk_code_lo:\n\ts_mov_b32 %1, fk_code_hi-fk_code_lo" : "=s"(pc), "=s"(len));
    asm volatile("buffer_load_dword %0, %1, %2, 0 offen" : "=&v"(pf) : "v"(tid >= 448 ? (tid - 448 + (blockIdx.x >> 3 & 3) * 64) * 128 : OOB), "s"(mk_rsrc((const void*)pc, len)) : "memory");
  }
  const int w = (int)rfl(tid >> 6);
  const int b = blockIdx.x, xcd = b & 7, loc = b >> 3;
  const u32 sb = rfl((u32)(size_t)(LDSP(char))smem), sb1 = sb + w * 1024;
  rsrc_t ws = mk_rsrc(p.ws, WS_END);
  const int kq = lane >> 4, l15 = lane & 15, qp = (lane >> 2) & 3, pp = lane & 3;
  u32 epv; ld32_sc1(epv, ws, WS_EP, 0);
  if (tid < 16) *(u32*)(s + L_CNT + tid * 4) = 0;
  const int r = (xcd >> 1) * 4 + (loc >> 3), ct = (xcd & 1) * 8 + (loc & 7);
  const int h = xcd * 4 + (loc & 3), gp = loc >> 2;
  const int sl = loc & 7, oi = loc >> 3;
  const int rot = ((loc >> 3) * 10 / 4 + (loc & 7) * 10 / 8 + xcd) % 10;
  u32 tag; u32x2 cand;
  const u32 candoff = WS_CAND + ((tid & 127) >> 3) * 128 + (tid & 7) * 8;
  if (ct < 15) g_phase(p, s, sb, ws, w, lane, r, ct, rot, xcd, loc, tag, epv, pf, cand, candoff STAMP_PASS);
  else r_phase(p, s, sb, ws, w, lane, tid, r, xcd, loc, tag, epv, pf, cand, candoff STAMP_PASS);

  STAMP(3, 22);
  if (w < 2) { PIN(cand); spin_gran(cand, ws, candoff, 0, tag); *(u32*)(s + L_KEYS + tid * 4) = cand[0]; }
  BAR();
  STAMP(22, 4);
  const bool f1 = loc < 30 && w < 4;
  u32x4 fw[4], xg[4];
  { const u32 a = L_F1W + (w >> 1 & 1) * 8192 + (8 * kq + qp) * 64 + (((w ^ kq) & 1) * 32) + pp * 8;
#pragma unroll
    for (int ks = 0; ks < 4; ++ks) fw[ks] = cat(trrd(s, a + ks * 2048), trrd(s, a + ks * 2048 + 256)); }
  { u32 key; const u32 rk = rank128(s, tid, key); if (rk < 8 && (tid & 3) == 0) *(u32*)(s + L_SEL + rk * 4) = key; }
  BAR();
  STAMP(4, 23);
  const u32 selkey = *(u32*)(s + L_SEL + (lane & 7) * 4), seltok = 2047 - (selkey & 0xffffu);
  rsrc_t rX = mk_rsrc(p.x, 2048 * 1280), rWo = mk_rsrc(p.wo, 640 * 1280);
  if (f1) {
    const u32 vo = seltok * 1280 + kq * 16, so = (loc % 5) * 256;
    ld128<0>(xg[0], rX, vo, so); ld128<64>(xg[1], rX, vo, so); ld128<128>(xg[2], rX, vo, so); ld128<192>(xg[3], rX, vo, so);
  }
  const u32 vwa = (lane >> 3) * 1280 + (((lane & 7) ^ (2 * ((kq & 1) | ((w & 1) << 1)))) * 16);
  const u32 vwb = ((lane >> 1 & 19) | (lane >> 2 & 4) | (lane & 8)) * 1280 + 128 + (lane & 1) * 16;
  const u32 swa = w * 10240 + sl * 160, swb = w * 40960 + sl * 160;
  auto wo_round = [&](auto ic) { constexpr int i = decltype(ic)::v;
    if (i < 7 || w < 4) dma<i * 8192>(rWo, vwa, swa + i * 81920, sb1);
    if ((w >> 1) == (i & 3) && (i < 7 || w == 6)) dma<L_WOB + (i >> 2) * 8192>(rWo, vwb, swb + (i >> 2) * 8 * 40960, sb1);
  };
  wo_round(IC<0>{}); wo_round(IC<1>{}); wo_round(IC<2>{});
  const int c0 = (5 * h) >> 1, krow = 2 * gp - 4 + w;
  const u32 fcol = ((c0 + 2 * (lane & 1)) >> 4) * 128;
  const u32 flq = WS_FG + (2 * gp + (w >> 2)) * 2048 + fcol, flk = WS_FG + krow * 2048 + (lane & 2 ? 1280 : 640) + fcol;
  const u32 fl1 = (lane < 4 && w < 6) ? flk : flq, fl0 = krow < 0 ? flq : fl1;
  u32 flv; ld32_sc1(flv, ws, fl0, 0);
  STAMP(23, 24);
  if (f1) WAITV(9); else if (w < 6) WAITV(5); else WAITV(4);
  arrive_flag(s, 1, ws, ct < 15 ? WS_FG + ((16 + r) * 16 + ct) * 128 : OOB, tag, lane);
  STAMP(24, 5);
  if (f1) {
    WAITV(6);
    f32x4 c = {0.f, 0.f, 0.f, 0.f};
#pragma unroll
    for (int ks = 0; ks < 4; ++ks) { PIN(xg[ks]); c = mfma(fw[ks], xg[ks], c); }
    const u32 po = WS_P1 + (((xcd * 6 + loc / 5) * 5 + loc % 5) * 2 + (w >> 1)) * 1024 + (w & 1) * 512 + kq * 128 + l15 * 16;
    st128_sc1(gran_pack(c, tag), ws, l15 < 8 ? po : OOB);
    WAITV(2);
  } else WAITV(1);
  PIN(flv); spin_flags(flv, ws, fl0, 0, tag);
  STAMP(5, 6);
  const u32 skv = WS_QKV + (80 + c0) * 32768 + krow * 1024;
  const u32 vq = (c0 + (kq > 2 ? 2 : kq)) * 32768 + (gp * 128 + w * 16 + l15) * 16;
  u32x4 q0, q1;
  if (w < 6) attn_stage<L_A0>(ws, sb1, krow < 0 ? OOB : skv, lane);
  ld128_sc1(q0, ws, vq, WS_QKV);
  ld32_sc1(flv, ws, fl1, 16 * 2048);
  STAMP(6, 25);
  WAITV(0); PIN(flv); spin_flags(flv, ws, fl1, 16 * 2048, tag);
  STAMP(25, 26);
  if (w < 6) attn_stage<L_A1>(ws, sb1, skv + 16 * 1024, lane);
  ld128_sc1(q1, ws, vq, WS_QKV + 1024 * 16);
  const int odd = h & 1;
  const u32x2 ones = {0x3f803f80u, 0x3f803f80u};
  const u32 aone = 18432 + (odd ? 0 : 12544 + 8) + w * 1024 + lane * 16;
  if (w < 6 && krow >= 0) *(u32x2*)(s + L_A0 + aone) = ones;
  const bool qlo = kq == 3 || (odd && kq == 0), qhi = kq == 3 || (!odd && kq == 2);
  PIN(q0); q0[0] = qlo ? 0u : q0[0]; q0[1] = qlo ? 0u : q0[1]; q0[2] = qhi ? 0u : q0[2]; q0[3] = qhi ? 0u : q0[3];
  const u32 vrow = L_A0 + 18432 + (16 * w + 4 * kq + qp) * 16;
  u32 aK = L_A0 + (kq > 2 ? 2 : kq) * 6144 + w * 256 + l15 * 16;
  u32 aV0 = vrow + ((pp + odd) >> 1) * 6272 + ((pp + odd) & 1) * 8;
  u32 aV1 = vrow + (pp == 0 ? 12544 + odd * 8 : (odd ? 0 : 12544 + 8));
  PIN(aK); PIN(aV0); PIN(aV1);
  u32 vo0 = (c0 + ((kq + odd) >> 1)) * 32768 + ((kq + odd) & 1) * 8 + l15 * 16, vo1 = kq == 0 ? (c0 + 2) * 32768 + odd * 8 + l15 * 16 : OOB;
  const u32 so = WS_O + (gp * 128 + w * 16) * 16;
  PIN(vo0); PIN(vo1);
  BAR();
  STAMP(26, 7);
  attn_unit(s, q0, aK, aV0, aV1, gp < 2 ? (16 - gp * 8 - w > 0 ? 16 - gp * 8 - w : 0) : 0, lane, ws, vo0, vo1, so,
            [&](auto bc) { constexpr int b = decltype(bc)::v; if constexpr (b < 5) wo_round(IC<b + 3>{}); });
  STAMP(7, 8);
  if (w < 4 || w == 6) WAITV(8); else WAITV(7);
  aK += L_A1 - L_A0; aV0 += L_A1 - L_A0; aV1 += L_A1 - L_A0; PIN(aK); PIN(aV0); PIN(aV1);
  if (w < 6) *(u32x2*)(s + L_A1 + aone) = ones;
  PIN(q1); q1[0] = qlo ? 0u : q1[0]; q1[1] = qlo ? 0u : q1[1]; q1[2] = qhi ? 0u : q1[2]; q1[3] = qhi ? 0u : q1[3];
  BAR();
  STAMP(8, 9);
  const int s64 = xcd * 3 + loc / 10, cgp = loc % 10;
  const bool f2 = loc < 30;
  u32x4 pp1[10];
  const u32 vp1 = WS_P1 + (2 * s64 + (w & 1)) * 10240 + lane * 16;
  auto p1load = [&]() { static_for<10>([&](auto ic) { constexpr int i = decltype(ic)::v; ld128_sc1<(i & 3) * 1024>(pp1[i], ws, vp1, (i >> 2) * 4096); }); };
  const int tokA = 128 * xcd + 32 * oi;
  const u32 fa = WS_FA + (xcd * 32 + (lane & 31)) * 128;
  const int tk = (tid * 6554) >> 16, pc = tid - tk * 10;
  u32x4 of[5], xv0, xv1; u32x2 g0, g1;
  const u32 xo = tid < 320 ? (tokA + tk) * 1280 + sl * 160 + pc * 16 : OOB, go = WS_GG + (tokA + (tk & 31)) * 8;
  attn_unit(s, q1, aK, aV0, aV1, 0, lane, ws, vo0, vo1, so + 1024 * 16, [&](auto bc) { constexpr int b = decltype(bc)::v;
    if constexpr (b == 0) dma<L_A0>(rWo, vwa, swa + 60 * 10240, sb1);
    if constexpr (b == 1) { dma<L_A0 + 8192>(rWo, vwa, swa + 68 * 10240, sb1); if (w < 5) dma<L_A0 + 20480>(rWo, vwb, swb + 15 * 40960, sb1); }
    if constexpr (b == 2) { if (w < 4) dma<L_A0 + 16384>(rWo, vwa, swa + 76 * 10240, sb1); if (w >= 6) p1load(); }
    if constexpr (b == 3) dma<L_F2W>(mk_rsrc(p.wd, 1536 * 1280), vwa, (s64 * 64 + 8 * w) * 1280 + cgp * 128, sb1);
    if constexpr (b == 4) {
      if (w < 4) WAITV(5); else if (w == 4) WAITV(4); else if (w == 5) WAITV(3); else WAITV(13);
      arrive_flag(s, 2, ws, WS_FA + (gp * 32 + h) * 128, tag, lane);
    }
    if constexpr (b == 5) { ld128(xv0, rX, xo, 0); ld128(xv1, rX, xo, 1024 * 1280); ld64_sc1(g0, ws, go, 0); ld64_sc1(g1, ws, go, 8192); }
  });
  STAMP(9, 10);
  WAITV(2);
  STAMP(10, 27);
  if (f2 && w >= 6) {
#pragma unroll 1
    for (int it = 0; it < SPIN_MAX; ++it) {
      bool ok = true;
#pragma unroll
      for (int i = 0; i < 10; ++i) { PIN(pp1[i]); ok = ok && pp1[i][3] == tag; }
      if (__all(ok)) break;
      p1load(); WAITV(0);
    }
    f32x4 g = gran_unpack(pp1[0]), u = gran_unpack(pp1[1]);
#pragma unroll
    for (int i = 1; i < 5; ++i) { g += gran_unpack(pp1[2 * i]); u += gran_unpack(pp1[2 * i + 1]); }
    u32x2 hv = {pk(g[0] * sigmoidf_(g[0]) * u[0], g[1] * sigmoidf_(g[1]) * u[1]), pk(g[2] * sigmoidf_(g[2]) * u[2], g[3] * sigmoidf_(g[3]) * u[3])};
    *(u32x2*)(s + L_H + (lane & 7) * 128 + (w & 1) * 64 + (lane >> 3) * 8) = hv;
  }
  BAR();
  STAMP(27, 28);
  const u32 fsw = ((qp >> 1) | ((kq & 1) << 1)) * 32 + pp * 8;
  if (f1) {
    const u32 a = L_F2W + (8 * kq + qp) * 128 + (fsw ^ (w * 32)), ah = L_H + (l15 & 7) * 128 + kq * 16;
    f32x4 c = {0.f, 0.f, 0.f, 0.f};
    c = mfma(cat(trrd(s, a), trrd(s, a + 512)), lds128(s, ah), c);
    c = mfma(cat(trrd(s, a + 4096), trrd(s, a + 4096 + 512)), lds128(s, ah + 64), c);
    st128_sc1(gran_pack(c, tag), ws, l15 < 8 ? WS_Y + ((s64 * 8 + l15) * 160 + cgp * 16 + w * 4 + kq) * 16 : OOB);
    WAITV(1);
  } else WAITV(0);
  arrive_flag(s, 3, ws, WS_FA + ((8 + gp) * 32 + h) * 128, tag, lane);
  ld32_sc1(flv, ws, fa, 0);
  STAMP(28, 11);
  u32x4 bw[5][5];
  { const int qd = w >> 1;
    const u32 a = (qd < 3 ? qd * 20480 : L_A0) + (8 * kq + qp) * 128;
    u32 ab = (qd < 3 ? L_WOB + qd * 5120 : L_A0 + 20480) + ((kq >> 1) * 16 + (kq & 1) * 4 + qp) * 32 + pp * 8, an[4];
    PIN(ab);
#pragma unroll
    for (int nt = 0; nt < 4; ++nt) { an[nt] = a + (fsw ^ (nt * 32)); PIN(an[nt]); }
#pragma unroll
    for (int ks = 0; ks < 5; ++ks) {
#pragma unroll
      for (int nt = 0; nt < 4; ++nt) bw[ks][nt] = cat(trrd(s, an[nt] + ks * 4096), trrd(s, an[nt] + ks * 4096 + 512));
      bw[ks][4] = cat(trrd(s, ab + ks * 1024), trrd(s, ab + ks * 1024 + 256));
    } }
  WAITV(0);
  PIN(flv); spin_flags(flv, ws, fa, 0, tag);
  STAMP(11, 12);
  const u32 ofo = (w >> 1) * 20 * 32768 + kq * 32768 + (tokA + (w & 1) * 16 + l15) * 16;
#pragma unroll
  for (int ks = 0; ks < 5; ++ks) ld128_sc1(of[ks], ws, ofo, WS_O + ks * 4 * 32768);
  PIN(g0); PIN(g1); PIN(xv0); PIN(xv1);
  spin_gran(g0, ws, go, 0, tag); spin_gran(g1, ws, go, 8192, tag);
  const float ga = __uint_as_float(g0[0]), gb = __uint_as_float(g1[0]);
  float ba[8], bb[8];
#pragma unroll
  for (int e = 0; e < 4; ++e) {
    ba[2 * e] = (1.f - ga) * blo(xv0[e]); ba[2 * e + 1] = (1.f - ga) * bhi(xv0[e]);
    bb[2 * e] = (1.f - gb) * blo(xv1[e]); bb[2 * e + 1] = (1.f - gb) * bhi(xv1[e]);
  }
  u32 ared = w * 5440 + (lane >> 5) * 544 + (lane & 31) * 16, afin = (tk >> 4) * 5440 + (pc >> 1) * 1088 + (pc & 1) * 544 + (tk & 15) * 16;
  u32 ared1 = ared + 43520, afin1 = afin + 43520;
  PIN(ared); PIN(afin); PIN(ared1); PIN(afin1);
  rsrc_t rO = mk_rsrc(p.out, 2048 * 1280);
  BAR();
  WAITV(0);
#pragma unroll
  for (int ks = 0; ks < 5; ++ks) PIN(of[ks]);
  ld32_sc1(flv, ws, fa, 256 * 128);
  o_compute(s, ared, bw, of);
  STAMP(12, 13);
  for (u32 m = (u32)__ballot(lane < 8 && ((seltok & 1023u) - (u32)tokA) < 32u); m; m &= m - 1) {
    const int c = __builtin_ctz(m);
    const u32 key = __builtin_amdgcn_readlane(selkey, c), tok = 2047 - (key & 0xffffu);
    const int sp = (tid * 3277) >> 16, c4 = tid - sp * 20;
    const u32 yo = WS_Y + (((sp < 24 ? sp : 0) * 8 + c) * 160 + sl * 20 + c4) * 16;
    u32x4 y;
#pragma unroll 1
    for (int it = 0; it < SPIN_MAX; ++it) { ld128_sc1(y, ws, yo, 0); WAITV(0); PIN(y); if (__all(y[3] == tag)) break; }
    *(f32x4*)(s + L_A1 + tid * 16) = gran_unpack(y);
    BAR();
    if (tid < 80) { float t = 0.f;
#pragma unroll
      for (int q = 0; q < 24; ++q) t += *(float*)(s + L_A1 + q * 320 + tid * 4);
      *(float*)(s + L_YS + tid * 4) = t; }
    BAR();
    if ((u32)(tokA + tk) == (tok & 1023u)) {
      const u32 o = key >> 16, rb = (o & 0x8000u) ? (o ^ 0x8000u) : (o ^ 0xffffu);
      const float rs = sigmoidf_(__uint_as_float(rb << 16));
      const f32x4 ya = *(f32x4*)(s + L_YS + pc * 32), yb = *(f32x4*)(s + L_YS + pc * 32 + 16);
      const float fa_ = tok < 1024 ? (1.f - ga) * rs : 0.f, fb_ = tok < 1024 ? 0.f : (1.f - gb) * rs;
#pragma unroll
      for (int e = 0; e < 4; ++e) { ba[e] += fa_ * ya[e]; ba[4 + e] += fa_ * yb[e]; bb[e] += fb_ * ya[e]; bb[4 + e] += fb_ * yb[e]; }
    }
  }
  BAR();
  if (b == 0 && w == 7) {
    u32 f[4];
#pragma unroll
    for (int i = 0; i < 4; ++i) ld32_sc1(f[i], ws, (lane & 15) < 15 ? WS_FG + (i * 64 + lane) * 128 : WS_CAND + (i * 4 + kq) * 128 + 4, 0);
    WAITV(0);
#pragma unroll
    for (int i = 0; i < 4; ++i) { PIN(f[i]); spin_flags(f[i], ws, (lane & 15) < 15 ? WS_FG + (i * 64 + lane) * 128 : WS_CAND + (i * 4 + kq) * 128 + 4, 0, tag); }
    if (lane == 0) st32_sc1(tag, ws, WS_EP);
  }
  WAITV(0); PIN(flv);
  const bool rdy = __all(flv == tag);
  if (!rdy) { o_final(s, afin, ga, ba, rO, xo, 0); spin_flags(flv, ws, fa, 256 * 128, tag); }
  STAMP(13, 14);
#pragma unroll
  for (int ks = 0; ks < 5; ++ks) ld128_sc1(of[ks], ws, ofo, WS_O + ks * 4 * 32768 + 1024 * 16);
  if (rdy) { o_final(s, afin, ga, ba, rO, xo, 0); WAITV(1); } else WAITV(0);
#pragma unroll
  for (int ks = 0; ks < 5; ++ks) PIN(of[ks]);
  STAMP(14, 15);
  o_compute(s, ared1, bw, of);
  BAR();
  STAMP(15, 16);
  if (w < 5) o_final(s, afin1, gb, bb, rO, xo, 1024 * 1280);
  STAMP(16, 17);
  asm volatile("s_waitcnt lgkmcnt(0)" ::: "memory"); STAMP(17, 31);
  if (tid == 0) __hip_atomic_store((u32*)(p.ws + WS_END + blockIdx.x * 128), (u32)__builtin_amdgcn_s_memrealtime(), __ATOMIC_RELAXED, __HIP_MEMORY_SCOPE_AGENT);
  asm volatile("fk_code_hi:" ::: "memory");
}

__global__ void tiny(char* ws) { const u32 t0 = (u32)__builtin_amdgcn_s_memrealtime();
  u32 d = t0 - __hip_atomic_load((u32*)(ws + WS_END + ((threadIdx.x & 31) * 8 + blockIdx.x) * 128), __ATOMIC_RELAXED, __HIP_MEMORY_SCOPE_AGENT);
  for (int o = 1; o < 32; o *= 2) { u32 e = __shfl_xor(d, o); d = e < d ? e : d; }
  d = rfl(d) * 0u;
  for (int i = 0; i < (1 << 20) && (u32)__builtin_amdgcn_s_memrealtime() - t0 < (d < 40000u ? d : 0u); ++i) __builtin_amdgcn_s_sleep(2); }
extern "C" void kernel_launch(void* const* d_in, const int* in_sizes, int n_in, void* d_out, int out_size, void* d_ws, size_t ws_size,
                              hipStream_t stream) {
  Params p;
  p.x = (const u16*)d_in[0]; p.wqkv = (const u16*)d_in[1]; p.wo = (const u16*)d_in[2]; p.wr = (const u16*)d_in[4];
  p.wg = (const u16*)d_in[5]; p.wu = (const u16*)d_in[6]; p.wd = (const u16*)d_in[7]; p.gw = (const u16*)d_in[8];
  p.out = (u16*)d_out; p.ws = (char*)d_ws;
  (void)hipFuncSetAttribute((const void*)fused_kernel, hipFuncAttributeMaxDynamicSharedMemorySize, LDS_BYTES);
  fused_kernel<<<256, 512, LDS_BYTES, stream>>>(p);
  tiny<<<8, 64, 0, stream>>>(p.ws);
}
```

```cpp
#include <hip/hip_runtime.h>
#include <hip/hip_bf16.h>

typedef unsigned short u16;
typedef unsigned int u32;
typedef unsigned long long u64;
typedef __attribute__((ext_vector_type(2))) u32 u32x2;
typedef __attribute__((ext_vector_type(4))) u32 u32x4;
typedef __attribute__((ext_vector_type(4))) float f32x4;
typedef __attribute__((ext_vector_type(8))) __bf16 bf16x8;
typedef __attribute__((ext_vector_type(2))) __bf16 bf16x2;
typedef __attribute__((ext_vector_type(4))) short s16x4;
#define LDSP(T) __attribute__((address_space(3))) T*
typedef __amdgpu_buffer_rsrc_t rsrc_t;
#define DEV __device__ __forceinline__

constexpr u32 WS_QKV = 0;
constexpr u32 WS_O   = WS_QKV + 240u * 32768;
constexpr u32 WS_P1  = WS_O + 80u * 32768;
constexpr u32 WS_Y   = WS_P1 + 480u * 1024;
constexpr u32 WS_FG  = WS_Y + 24u * 8 * 160 * 16;
constexpr u32 WS_FA  = WS_FG + 512u * 128;
constexpr u32 WS_CAND = WS_FA + 512u * 128;
constexpr u32 WS_GG  = WS_CAND + 16u * 128;
constexpr u32 WS_EP  = WS_GG + 2048u * 8;
constexpr u32 WS_END = WS_EP + 128;
constexpr u32 WS_STAMP = WS_END;

constexpr u32 L_X1   = 73728;
constexpr u32 L_F1W  = 51200;
constexpr u32 L_WOB  = 61440;
constexpr u32 L_A0   = 76800;
constexpr u32 L_A1   = 114048;
constexpr u32 L_F2W  = 151296;
constexpr u32 L_RW   = 155648;
constexpr u32 L_CNT  = 159488;
constexpr u32 L_SEL  = 159552;
constexpr u32 L_H    = 159744;
constexpr u32 L_KEYS = 160768;
constexpr u32 L_RB   = 161280;
constexpr u32 L_YS   = 161280;
constexpr u32 LDS_BYTES = 163840;
constexpr int SPIN_MAX = 1 << 14;
constexpr u32 OOB = 0x80000000u;

struct Params {
  const u16* x; const u16* wqkv; const u16* wo; const u16* wr; const u16* wg; const u16* wu; const u16* wd; const u16* gw;
  u16* out; char* ws;
};

#define STAMP(prev, i) do {} while (0)
#define STAMP_ARG
#define STAMP_PASS

DEV rsrc_t mk_rsrc(const void* p, u32 bytes) { return __builtin_amdgcn_make_buffer_rsrc((void*)p, 0, bytes, 0x00020000); }
DEV u32 rfl(u32 v) { return (u32)__builtin_amdgcn_readfirstlane((int)v); }
template <u32 L> DEV void dma(rsrc_t r, u32 voff, u32 soff, u32 lds) {
  asm volatile("s_add_u32 m0, %0, %4\n\ts_nop 0\n\tbuffer_load_dwordx4 %1, %2, %3 offen lds" :: "s"(lds), "v"(voff), "s"(r), "s"(soff), "n"(L) : "memory", "scc");
}
template <u32 L> DEV void dma_sc1(rsrc_t r, u32 voff, u32 soff, u32 lds) {
  asm volatile("s_add_u32 m0, %0, %4\n\ts_nop 0\n\tbuffer_load_dwordx4 %1, %2, %3 offen sc1 lds" :: "s"(lds), "v"(voff), "s"(r), "s"(soff), "n"(L) : "memory", "scc");
}
template <u32 L> DEV void dma3(rsrc_t rX, rsrc_t rW, u32 vX, u32 vW0, u32 vW1, u32 sX, u32 sW, u32 lds) {
  asm volatile("s_add_u32 m0, %0, %8\n\ts_nop 0\n\tbuffer_load_dwordx4 %1, %2, %3 offen lds\n\t"
               "buffer_load_dwordx4 %4, %6, %7 offen offset:1024 lds\n\tbuffer_load_dwordx4 %5, %6, %7 offen offset:2048 lds"
               :: "s"(lds), "v"(vX), "s"(rX), "s"(sX), "v"(vW0), "v"(vW1), "s"(rW), "s"(sW), "n"(L) : "memory", "scc");
}
template <u32 I> DEV void dma_cont(rsrc_t r, u32 voff, u32 soff) {
  asm volatile("buffer_load_dwordx4 %0, %1, %2 offen offset:%3 lds" :: "v"(voff), "s"(r), "s"(soff), "n"(I) : "memory");
}
template <u32 I = 0> DEV void ld128_sc1(u32x4& d, rsrc_t r, u32 voff, u32 soff) { asm volatile("buffer_load_dwordx4 %0, %1, %2, %3 offen offset:%4 sc1" : "=&v"(d) : "v"(voff), "s"(r), "s"(soff), "n"(I) : "memory"); }
template <u32 I = 0> DEV void ld128(u32x4& d, rsrc_t r, u32 voff, u32 soff) { asm volatile("buffer_load_dwordx4 %0, %1, %2, %3 offen offset:%4" : "=&v"(d) : "v"(voff), "s"(r), "s"(soff), "n"(I) : "memory"); }
template <u32 I> DEV void gld128(u32x4& d, const char* a) { asm volatile("global_load_dwordx4 %0, %1, off offset:%2" : "=&v"(d) : "v"(a), "n"(I) : "memory"); }
DEV void ld64_sc1(u32x2& d, rsrc_t r, u32 voff, u32 soff) { asm volatile("buffer_load_dwordx2 %0, %1, %2, %3 offen sc1" : "=&v"(d) : "v"(voff), "s"(r), "s"(soff) : "memory"); }
DEV void ld32_sc1(u32& d, rsrc_t r, u32 voff, u32 soff) { asm volatile("buffer_load_dword %0, %1, %2, %3 offen sc1" : "=&v"(d) : "v"(voff), "s"(r), "s"(soff) : "memory"); }
#define WAITV(n) asm volatile("s_waitcnt vmcnt(%0)" :: "n"(n) : "memory")
template <class T> DEV void pin_(T& x) { asm volatile("" : "+v"(x)); }
#define PIN(x) pin_(x)
#define BAR() do { asm volatile("" ::: "memory"); __builtin_amdgcn_s_waitcnt(0xC07F); asm volatile("s_barrier" ::: "memory"); } while (0)
#define SCHED() __builtin_amdgcn_sched_barrier(0)
DEV void st64_sc1(u32x2 v, rsrc_t r, u32 voff, u32 soff = 0) { __builtin_amdgcn_raw_buffer_store_b64(v, r, voff, soff, 16); }
DEV void st128_sc1(u32x4 v, rsrc_t r, u32 voff) { __builtin_amdgcn_raw_buffer_store_b128(v, r, voff, 0, 16); }
DEV void st32_sc1(u32 v, rsrc_t r, u32 voff) { __builtin_amdgcn_raw_buffer_store_b32(v, r, voff, 0, 16); }

DEV u32 pk(float a, float b) { bf16x2 v = {(__bf16)a, (__bf16)b}; return __builtin_bit_cast(u32, v); }
DEV float rbf(float a) { return (float)(__bf16)a; }
DEV float blo(u32 v) { return __uint_as_float(v << 16); }
DEV float bhi(u32 v) { return __uint_as_float(v & 0xffff0000u); }
DEV f32x4 mfma(u32x4 a, u32x4 b, f32x4 c) { return __builtin_amdgcn_mfma_f32_16x16x32_bf16(__builtin_bit_cast(bf16x8, a), __builtin_bit_cast(bf16x8, b), c, 0, 0, 0); }
DEV u32x2 trrd(char* s, u32 a) { return __builtin_bit_cast(u32x2, __builtin_amdgcn_ds_read_tr16_b64_v4i16((LDSP(s16x4))(s + a))); }
DEV u32x4 lds128(char* s, u32 a) { return *(u32x4*)(s + a); }
DEV u32x4 cat(u32x2 a, u32x2 b) { u32x4 r = {a[0], a[1], b[0], b[1]}; return r; }
DEV float ex2(float v) { return __builtin_amdgcn_exp2f(v); }
DEV float sigmoidf_(float v) { return __builtin_amdgcn_rcpf(1.f + ex2(v * -1.4426950408889634f)); }
DEV u32x4 gran_pack(f32x4 c, u32 tag) {
  const u32 a = __float_as_uint(c[0]), b = __float_as_uint(c[1]), d = __float_as_uint(c[2]), e = __float_as_uint(c[3]);
  u32x4 r = {__builtin_amdgcn_perm(a, e, 0x07060503u), __builtin_amdgcn_perm(b, e, 0x07060502u), __builtin_amdgcn_perm(d, e, 0x07060501u), tag};
  return r;
}
DEV f32x4 gran_unpack(u32x4 g) {
  const u32 t = __builtin_amdgcn_perm(g[0], g[1], 0x04000c0cu);
  f32x4 r = {__uint_as_float(g[0]), __uint_as_float(g[1]), __uint_as_float(g[2]), __uint_as_float(__builtin_amdgcn_perm(t, g[2], 0x0706000cu))};
  return r;
}

DEV void arrive_flag(char* s, int cnt, rsrc_t ws, u32 flagoff, u32 tag, int lane) {
  if (lane == 0) {
    u32 old;
    asm volatile("ds_add_rtn_u32 %0, %1, %2\n\ts_waitcnt lgkmcnt(0)" : "=v"(old) : "v"((u32)(size_t)(LDSP(char))(s + L_CNT + cnt * 4)), "v"(1u) : "memory");
    st32_sc1(tag, ws, old == 7 ? flagoff : OOB);
  }
}
DEV void spin_flags(u32 v, rsrc_t ws, u32 voff, u32 soff, u32 tag) {
#pragma unroll 1
  for (int i = 0; i < SPIN_MAX && !__all(v == tag); ++i) { ld32_sc1(v, ws, voff, soff); WAITV(0); PIN(v); }
}
DEV void spin_gran(u32x2& v, rsrc_t ws, u32 voff, u32 soff, u32 tag) {
#pragma unroll 1
  for (int i = 0; i < SPIN_MAX && !__all(v[1] == tag); ++i) { ld64_sc1(v, ws, voff, soff); WAITV(0); PIN(v); }
}
DEV u32 rank128(char* s, int tid, u32& key) {
  key = *(u32*)(s + L_KEYS + (tid >> 2) * 4);
  u32 c = 0;
#pragma unroll
  for (int i = 0; i < 8; ++i) {
    u32x4 k = lds128(s, L_KEYS + (tid & 3) * 128 + i * 16);
    c += (k[0] > key) + (k[1] > key) + (k[2] > key) + (k[3] > key);
  }
  c += __builtin_amdgcn_update_dpp(0u, c, 0xB1, 0xf, 0xf, true);
  c += __builtin_amdgcn_update_dpp(0u, c, 0x4E, 0xf, 0xf, true);
  return c;
}
DEV void f1w_stage(const Params& p, u32 sb, int w, int lane, int xcd, int loc) {
  const int s32 = xcd * 6 + (loc < 30 ? loc / 5 : 0), kr = loc % 5;
  const u32 vo = (16 * w + (lane >> 2)) * 3072 + (((lane & 3) ^ ((lane >> 5) * 2)) * 16), so = kr * (128 * 3072) + s32 * 64;
  dma<L_F1W>(mk_rsrc(p.wg, 640 * 1536 * 2), vo, so, sb + w * 1024);
  dma<L_F1W + 8192>(mk_rsrc(p.wu, 640 * 1536 * 2), vo, so, sb + w * 1024);
}

DEV void g_epilogue(f32x4 (&acc)[2][2], rsrc_t ws, u32 voff, u32 soff, bool isq) {
  if (isq) {
#pragma unroll
    for (int i = 0; i < 4; ++i) acc[i >> 1][i & 1] *= 0.32259863752095353f;
  }
#pragma unroll
  for (int ft = 0; ft < 2; ++ft)
#pragma unroll
    for (int tt = 0; tt < 2; ++tt) {
      f32x4 a = acc[ft][tt];
      u32x2 v = {pk(a[0], a[1]), pk(a[2], a[3])};
      st64_sc1(v, ws, voff + tt * 256, soff + ft * 65536);
    }
}
constexpr u32 ring_slot(int i) { return (i & 3) == 3 ? L_X1 + 7 * 8192 : (i & 3) * 24576; }
template <int I> struct IC { static constexpr int v = I; };
template <int N, class F> DEV void static_for(F&& f) { if constexpr (N > 0) { static_for<N - 1>(f); f(IC<N - 1>{}); } }

DEV void g_phase(const Params& p, char* s, u32 sb, rsrc_t ws, int w, int lane, int r, int ct, int rot, int xcd, int loc,
                 u32& tag, u32 epv, u32 pf, u32x2& cand, u32 candoff STAMP_ARG) {
  const int cg = w & 3, th = w >> 2, kq = lane >> 4, l15 = lane & 15;
  rsrc_t rW = mk_rsrc((const char*)p.wqkv - 2048, 640 * 3840 + 2048), rX = mk_rsrc(p.x, 2048 * 1280);
  const u32 vW = kq * 3840 + ((l15 ^ (2 * (kq | ((w & 1) << 2)))) * 16),
    vW0 = vW + 1024, vW1 = vW + 4 * 3840;
  const u32 vX = (lane >> 3) * 1280 + (((lane & 7) ^ ((4 * w + kq) & 7)) * 16);
  const u32 sW = 8 * w * 3840 + ct * 256, sX = (64 * r + 8 * w) * 1280, sb3 = sb + w * 3072, sb1 = sb + w * 1024;
  u32 k0 = rot * 128, k1 = rot * 128;
  auto batch = [&](auto ic) { constexpr int i = decltype(ic)::v;
    dma3<ring_slot(i)>(rX, rW, vX, vW0, vW1, sX + k0, sW + k0 * 1920, sb3); k0 = k0 == 1152 ? 0 : k0 + 128; };
  auto x1 = [&](auto jc) { constexpr int j = decltype(jc)::v;
    dma<L_X1 + j * 8192>(rX, vX, sX + k1 + 1024 * 1280, sb1); k1 = k1 == 1152 ? 0 : k1 + 128; };
  STAMP(0, 18);
  batch(IC<0>{});
  asm volatile("s_barrier" ::: "memory");
  batch(IC<1>{});
  asm volatile("s_barrier" ::: "memory");
  batch(IC<2>{});
  STAMP(18, 19);
  const int qp = (lane >> 2) & 3, pp = lane & 3, hrow = qp | ((kq & 1) << 2);
  u32 aW0 = kq * 3072 + 1024 + qp * 256 + (((cg * 4 + (pp >> 1)) ^ (2 * hrow)) * 16) + (pp & 1) * 8, aW1 = aW0 ^ 32;
  const u32 xsw = (kq ^ (l15 >> 1)) * 16;
  u32 aR0 = (4 * th + (l15 >> 3)) * 3072 + (l15 & 7) * 128 + xsw, aR1 = aR0 ^ 64;
  u32 aS0 = L_X1 + (32 * th + l15) * 128 + xsw, aS1 = aS0 ^ 64;
  PIN(aW0); PIN(aW1); PIN(aR0); PIN(aR1); PIN(aS0); PIN(aS1);
  u32x4 wf[20][2];
  f32x4 acc[2][2] = {}, acd[2][2] = {};
  u32x4 xf[2][4], xh[10][4];
  auto mm0 = [&](auto ic, int h) { constexpr int i = decltype(ic)::v;
#pragma unroll
    for (int q = 0; q < 4; ++q) acc[q & 1][q >> 1] = mfma(wf[2 * i + h][q & 1], xf[i & 1][2 * h + (q >> 1)], acc[q & 1][q >> 1]); };
  auto rd1 = [&](int j) {
#pragma unroll
    for (int q = 0; q < 4; ++q) xh[j][q] = lds128(s, ((q >> 1) ? aS1 : aS0) + (j < 6 ? j : j - 6) * 8192 + (q & 1) * 2048); };
  auto mm1 = [&](int j) {
#pragma unroll
    for (int q = 0; q < 8; ++q) acd[q & 1][q >> 1 & 1] = mfma(wf[2 * j + (q >> 2)][q & 1], xh[j][q >> 1], acd[q & 1][q >> 1 & 1]); };
  static_for<10>([&](auto ic) { constexpr int i = decltype(ic)::v;
    constexpr u32 base = (i & 3) == 3 ? 0 : ring_slot(i), jump = (i & 3) == 3 ? ring_slot(3) : (i & 3) == 0 && i ? -ring_slot(3) : 0;
    if constexpr (jump != 0) { aW0 += jump; aW1 += jump; aR0 += jump; aR1 += jump; PIN(aW0); PIN(aW1); PIN(aR0); PIN(aR1); }
    const u32 kx = sX + k0, kw = sW + k0 * 1920;
    k0 = k0 == 1152 ? 0 : k0 + 128;
    WAITV(i < 8 ? 6 : i == 8 ? 5 : 4);
    BAR();
    if constexpr (i < 7) dma<ring_slot(i + 3)>(rX, vX, kx, sb3); else x1(IC<2 * i - 14>{});
    if (i == 0) { PIN(epv); PIN(pf); tag = rfl(epv) * 1664525u + 1013904223u; STAMP(19, 20); }
#pragma unroll
    for (int ks = 0; ks < 2; ++ks) {
      wf[2 * i + ks][0] = cat(trrd(s, base + aW0 + ks * 12288), trrd(s, base + aW0 + ks * 12288 + 1024));
      xf[i & 1][2 * ks] = lds128(s, base + (ks ? aR1 : aR0));
      wf[2 * i + ks][1] = cat(trrd(s, base + aW1 + ks * 12288), trrd(s, base + aW1 + ks * 12288 + 1024));
      xf[i & 1][2 * ks + 1] = lds128(s, base + (ks ? aR1 : aR0) + 6144);
    }
    SCHED();
    if constexpr (i > 0) mm0(IC<i - 1>{}, 0);
    SCHED();
    if constexpr (i < 7) dma_cont<1024>(rW, vW0, kw);
    SCHED();
    if constexpr (i > 0) mm0(IC<i - 1>{}, 1);
    SCHED();
    if constexpr (i < 7) dma_cont<2048>(rW, vW1, kw); else x1(IC<2 * i - 13>{});
  });
  mm0(IC<9>{}, 0); mm0(IC<9>{}, 1);
  STAMP(20, 1);
  const u32 vst = (kq >> 1) * 32768 + l15 * 16 + (kq & 1) * 8, sst = WS_QKV + (ct * 16 + cg * 4) * 32768 + (64 * r + 32 * th) * 16;
  g_epilogue(acc, ws, vst, sst, ct < 5);
  WAITV(4);
  BAR();
  STAMP(1, 2);
  rd1(0); rd1(1); SCHED();
  x1(IC<6>{}); rd1(2); mm1(0); SCHED();
  x1(IC<7>{}); rd1(3); mm1(1); SCHED();
  x1(IC<8>{}); rd1(4); mm1(2); SCHED();
  x1(IC<9>{}); rd1(5); mm1(3); SCHED();
  aS0 += 6 * 8192; aS1 += 6 * 8192; PIN(aS0); PIN(aS1);
  mm1(4); SCHED();
  WAITV(0);
  arrive_flag(s, 0, ws, WS_FG + (r * 16 + ct) * 128, tag, lane);
  BAR();
  STAMP(2, 21);
  rd1(6); rd1(7);
  if (w < 2) ld64_sc1(cand, ws, candoff, 0);
  mm1(5); SCHED();
  f1w_stage(p, sb, w, lane, xcd, loc);
  rd1(8); mm1(6); SCHED();
  rd1(9); mm1(7); SCHED();
  mm1(8); mm1(9);
  STAMP(21, 3);
  g_epilogue(acd, ws, vst, sst + 1024 * 16, ct < 5);
  WAITV(4);
}

DEV void r_phase(const Params& p, char* s, u32 sb, rsrc_t ws, int w, int lane, int tid, int r, int xcd, int loc,
                 u32& tag, u32 epv, u32 pf, u32x2& cand, u32 candoff STAMP_ARG) {
  const int kq = lane >> 4, l15 = lane & 15;
  rsrc_t rX = mk_rsrc(p.x, 2048 * 1280);
  const u32 vX = (lane >> 3) * 1280 + (((lane & 7) ^ ((4 * w + kq) & 7)) * 16), sX = (64 * r + 8 * w) * 1280, sb1 = sb + w * 1024;
  static_for<9>([&](auto ic) { constexpr int i = decltype(ic)::v; dma<i * 8192>(rX, vX, sX + i * 128, sb1); });
  static_for<10>([&](auto ic) { constexpr int i = decltype(ic)::v; dma<L_X1 + i * 8192>(rX, vX, sX + i * 128 + 1024 * 1280, sb1); });
  if ((w & 1) * 1024 + lane * 16 < 1280) {
    if (w < 2) dma<L_RW>(mk_rsrc(p.wr, 1280), w * 1024 + lane * 16, 0, sb1);
    else if (w < 4) dma<L_RW + 1536 - 2048>(mk_rsrc(p.gw, 1280), (w - 2) * 1024 + lane * 16, 0, sb1);
  }
  u32x4 xa, xb;
  ld128<1152>(xa, rX, (64 * r + 16 * (w & 3) + l15) * 1280 + kq * 16, 0); ld128<1216>(xb, rX, (64 * r + 16 * (w & 3) + l15) * 1280 + kq * 16, 0);
  u32 aw = L_RW + (l15 == 1 ? 1536 : 0) + kq * 16;
  u32 aX0 = ((w >> 2) ? L_X1 : 0) + (16 * (w & 3) + l15) * 128 + ((kq ^ (l15 >> 1)) * 16), aX1 = aX0 ^ 64, aX2 = aX0 + 65536, aX3 = aX1 + 65536;
  PIN(aw); PIN(aX0); PIN(aX1); PIN(aX2); PIN(aX3);
  f32x4 acc = {0.f, 0.f, 0.f, 0.f};
  WAITV(0); BAR();
  STAMP(0, 29);
  PIN(epv); PIN(pf); PIN(xa); PIN(xb); tag = rfl(epv) * 1664525u + 1013904223u;
  if (w >> 2) { xa = lds128(s, 8192 + aX2); xb = lds128(s, 8192 + aX3); }
  u32x4 wf[20], xr[18];
#pragma unroll
  for (int i = 0; i < 9; ++i) {
    wf[2 * i] = lds128(s, aw + i * 128); xr[2 * i] = lds128(s, i < 8 ? i * 8192 + aX0 : aX2);
    wf[2 * i + 1] = lds128(s, aw + i * 128 + 64); xr[2 * i + 1] = lds128(s, i < 8 ? i * 8192 + aX1 : aX3);
  }
  wf[18] = lds128(s, aw + 1152); wf[19] = lds128(s, aw + 1216);
  SCHED();
#pragma unroll
  for (int i = 0; i < 18; ++i) acc = mfma(xr[i], wf[i], acc);
  acc = mfma(xa, wf[18], acc);
  acc = mfma(xb, wf[19], acc);
  if (l15 < 2) *(f32x4*)(s + L_RB + l15 * 512 + (16 * w + 4 * kq) * 4) = acc;
  BAR();
  STAMP(29, 30);
  u32x2 gr;
  if (tid < 128) {
    const int tok = (tid >> 6) * 1024 + 64 * r + (tid & 63);
    const float rv = *(float*)(s + L_RB + tid * 4), gl = *(float*)(s + L_RB + 512 + tid * 4);
    const u32 rb = pk(rv, 0.f) & 0xffffu;
    *(u32*)(s + L_KEYS + tid * 4) = ((rb ^ ((rb & 0x8000u) ? 0xffffu : 0x8000u)) << 16) | (u32)(2047 - tok);
    gr[0] = __float_as_uint(sigmoidf_(gl)); gr[1] = tag;
  }
  BAR();
  u32 key; const u32 rk = rank128(s, tid, key);
  if (rk < 8 && (tid & 3) == 0) { u32x2 c = {key, tag}; st64_sc1(c, ws, WS_CAND + r * 128 + rk * 8); }
  if (tid < 128) st64_sc1(gr, ws, WS_GG + ((tid >> 6) * 1024 + 64 * r + (tid & 63)) * 8);
  f1w_stage(p, sb, w, lane, xcd, loc);
  STAMP(30, 3);
  BAR();
  if (w < 2) ld64_sc1(cand, ws, candoff, 0);
  WAITV(0);
}

template <u32 BUF> DEV void attn_stage(rsrc_t ws, u32 sb1, u32 so, int lane) {
  dma_sc1<BUF>(ws, lane * 16, so, sb1);
  dma_sc1<BUF + 6144>(ws, lane * 16, so + 32768, sb1);
  dma_sc1<BUF + 12288>(ws, lane * 16, so + 65536, sb1);
  dma_sc1<BUF + 18432>(ws, lane * 16, so + 80 * 32768, sb1);
  dma_sc1<BUF + 18432 + 6272>(ws, lane * 16, so + 81 * 32768, sb1);
  dma_sc1<BUF + 18432 + 12544>(ws, lane * 16, so + 82 * 32768, sb1);
}
DEV float xmax(float m) {
  auto a = __builtin_amdgcn_permlane16_swap(__float_as_uint(m), __float_as_uint(m), false, false);
  m = fmaxf(__uint_as_float(a[0]), __uint_as_float(a[1]));
  auto b = __builtin_amdgcn_permlane32_swap(__float_as_uint(m), __float_as_uint(m), false, false);
  return fmaxf(__uint_as_float(b[0]), __uint_as_float(b[1]));
}
template <class Mid>
DEV void attn_unit(char* s, u32x4 qf, u32 aK, u32 aV0, u32 aV1, int nskip, int lane, rsrc_t ws, u32 vo0, u32 vo1, u32 so, Mid&& mid) {
  const int kq = lane >> 4, l15 = lane & 15, dg = l15 - 4 * kq;
  const float NEG = -__builtin_inff();
  const f32x4 z = {0.f, 0.f, 0.f, 0.f};
  const u32x2 zz = {0u, 0u};
  u32x4 kf[18], va[9], vb[9];
  kf[16] = lds128(s, aK + 16 * 256); kf[0] = lds128(s, aK); kf[1] = lds128(s, aK + 256);
  va[8] = cat(trrd(s, aV0 + 16 * 256), zz); vb[8] = cat(trrd(s, aV1 + 16 * 256), zz);
  kf[2] = lds128(s, aK + 512); kf[3] = lds128(s, aK + 768);
  va[0] = cat(trrd(s, aV0), trrd(s, aV0 + 256)); vb[0] = cat(trrd(s, aV1), trrd(s, aV1 + 256));
  SCHED();
  f32x4 sd = mfma(kf[16], qf, z);
#pragma unroll
  for (int e = 0; e < 4; ++e) sd[e] = e <= dg ? sd[e] : NEG;
  const float m = xmax(fmaxf(fmaxf(sd[0], sd[1]), fmaxf(sd[2], sd[3])));
  const f32x4 c = {-m, -m, -m, -m};
  f32x4 sa = mfma(kf[0], qf, c), sc = mfma(kf[1], qf, c);
  u32x4 P = {pk(ex2(sd[0] - m), ex2(sd[1] - m)), pk(ex2(sd[2] - m), ex2(sd[3] - m)), 0u, 0u};
  f32x4 o0 = mfma(va[8], P, z), o1 = mfma(vb[8], P, z);
#pragma unroll
  for (int e = 0; e < 4; ++e) sa[e] = e > dg ? sa[e] : NEG;
  static_for<8>([&](auto bc) { constexpr int b = decltype(bc)::v;
    SCHED();
    f32x4 na, nc;
    if (b < 7) na = mfma(kf[2 * b + 2], qf, c);
    if (b < 6) { kf[2 * b + 4] = lds128(s, aK + (2 * b + 4) * 256); kf[2 * b + 5] = lds128(s, aK + (2 * b + 5) * 256); }
    P[0] = pk(ex2(sa[0]), ex2(sa[1])); P[1] = pk(ex2(sa[2]), ex2(sa[3]));
    SCHED();
    if (b < 7) nc = mfma(kf[2 * b + 3], qf, c);
    if (b < 7) {
      va[b + 1] = cat(trrd(s, aV0 + (2 * b + 2) * 256), trrd(s, aV0 + (2 * b + 3) * 256));
      vb[b + 1] = cat(trrd(s, aV1 + (2 * b + 2) * 256), trrd(s, aV1 + (2 * b + 3) * 256));
    }
    P[2] = pk(ex2(sc[0]), ex2(sc[1])); P[3] = pk(ex2(sc[2]), ex2(sc[3]));
    SCHED();
    o0 = mfma(va[b], P, o0);
    o1 = mfma(vb[b], P, o1);
    mid(bc);
    sa = na; sc = nc;
  });
  SCHED();
  auto sw = __builtin_amdgcn_permlane16_swap(__float_as_uint(o1[0]), __float_as_uint(o1[0]), false, false);
  float l = kq == 0 ? __uint_as_float(sw[1]) : o1[0];
  {
    const float big = fmaxf(fmaxf(fmaxf(fabsf(o0[0]), fabsf(o0[1])), fmaxf(fabsf(o0[2]), fabsf(o0[3]))),
                            fmaxf(fmaxf(fabsf(o1[0]), fabsf(o1[1])), fmaxf(fabsf(o1[2]), fabsf(o1[3]))));
    const float sum = (o0[0] + o0[1]) + (o0[2] + o0[3]) + (o1[0] + o1[1]) + (o1[2] + o1[3]);
    const bool ok = l < 1.8446744073709552e19f && big < 1e37f && sum == sum;
    if (__builtin_expect(!__all(ok), 0)) {
      float mt = m;
#pragma unroll 1
      for (int t = nskip; t < 16; ++t) {
        f32x4 v = mfma(lds128(s, aK + t * 256), qf, z);
#pragma unroll
        for (int e = 0; e < 4; ++e) mt = fmaxf(mt, (t > 0 || e > dg) ? v[e] : NEG);
      }
      mt = xmax(mt);
      o0 = z; o1 = z;
#pragma unroll 1
      for (int t = nskip; t < 17; ++t) {
        f32x4 v = mfma(lds128(s, aK + t * 256), qf, z);
#pragma unroll
        for (int e = 0; e < 4; ++e) v[e] = (t == 0 ? e > dg : (t < 16 || e <= dg)) ? ex2(v[e] - mt) : 0.f;
        const u32x4 Q = {pk(v[0], v[1]), pk(v[2], v[3]), 0u, 0u};
        o0 = mfma(cat(trrd(s, aV0 + t * 256), zz), Q, o0);
        o1 = mfma(cat(trrd(s, aV1 + t * 256), zz), Q, o1);
      }
      auto s2 = __builtin_amdgcn_permlane16_swap(__float_as_uint(o1[0]), __float_as_uint(o1[0]), false, false);
      l = kq == 0 ? __uint_as_float(s2[1]) : o1[0];
    }
  }
  const float inv = __builtin_amdgcn_rcpf(l);
  u32x2 v0 = {pk(o0[0] * inv, o0[1] * inv), pk(o0[2] * inv, o0[3] * inv)};
  u32x2 v1 = {pk(o1[0] * inv, o1[1] * inv), pk(o1[2] * inv, o1[3] * inv)};
  st64_sc1(v0, ws, vo0, so);
  st64_sc1(v1, ws, vo1, so);
}

DEV void o_compute(char* s, u32 a, u32x4 (&bw)[5][5], u32x4 (&of)[5]) {
  f32x4 acc[5] = {};
#pragma unroll
  for (int ks = 0; ks < 5; ++ks)
#pragma unroll
    for (int nt = 0; nt < 5; ++nt) acc[nt] = mfma(bw[ks][nt], of[ks], acc[nt]);
#pragma unroll
  for (int nt = 0; nt < 5; ++nt) *(f32x4*)(s + a + nt * 1088) = acc[nt];
}
DEV void o_final(char* s, u32 a, float g, const float (&b)[8], rsrc_t rO, u32 vo, u32 so) {
  f32x4 lo = *(f32x4*)(s + a), hi = *(f32x4*)(s + a + 256);
#pragma unroll
  for (int q = 1; q < 4; ++q) { lo += *(f32x4*)(s + a + q * 10880); hi += *(f32x4*)(s + a + q * 10880 + 256); }
  u32x4 o = {pk(g * lo[0] + b[0], g * lo[1] + b[1]), pk(g * lo[2] + b[2], g * lo[3] + b[3]),
             pk(g * hi[0] + b[4], g * hi[1] + b[5]), pk(g * hi[2] + b[6], g * hi[3] + b[7])};
  __builtin_amdgcn_raw_buffer_store_b128(o, rO, vo, so, 0);
}

__global__ __launch_bounds__(512, 2) void fused_kernel(Params p) {
  extern __shared__ __attribute__((aligned(16))) char smem[];
  char* s = smem;
  const int tid = threadIdx.x, lane = tid & 63;
  u32 pf;
  {
    u64 pc; u32 len;
    asm volatile("s_getpc_b64 %0\nfk_code_lo:\n\ts_mov_b32 %1, fk_code_hi-fk_code_lo" : "=s"(pc), "=s"(len));
    asm volatile("buffer_load_dword %0, %1, %2, 0 offen" : "=&v"(pf) : "v"(tid >= 448 ? (tid - 448 + (blockIdx.x >> 3 & 3) * 64) * 128 : OOB), "s"(mk_rsrc((const void*)pc, len)) : "memory");
  }
  const int w = (int)rfl(tid >> 6);
  const int b = blockIdx.x, xcd = b & 7, loc = b >> 3;
  const u32 sb = rfl((u32)(size_t)(LDSP(char))smem), sb1 = sb + w * 1024;
  rsrc_t ws = mk_rsrc(p.ws, WS_END);
  const int kq = lane >> 4, l15 = lane & 15, qp = (lane >> 2) & 3, pp = lane & 3;
  u32 epv; ld32_sc1(epv, ws, WS_EP, 0);
  if (tid < 16) *(u32*)(s + L_CNT + tid * 4) = 0;
  const int r = (xcd >> 1) * 4 + (loc >> 3), ct = (xcd & 1) * 8 + (loc & 7);
  const int h = xcd * 4 + (loc & 3), gp = loc >> 2;
  const int sl = loc & 7, oi = loc >> 3;
  const int rot = ((loc >> 3) * 10 / 4 + (loc & 7) * 10 / 8 + xcd) % 10;
  u32 tag; u32x2 cand;
  const u32 candoff = WS_CAND + ((tid & 127) >> 3) * 128 + (tid & 7) * 8;
  if (ct < 15) g_phase(p, s, sb, ws, w, lane, r, ct, rot, xcd, loc, tag, epv, pf, cand, candoff STAMP_PASS);
  else r_phase(p, s, sb, ws, w, lane, tid, r, xcd, loc, tag, epv, pf, cand, candoff STAMP_PASS);

  STAMP(3, 22);
  if (w < 2) { PIN(cand); spin_gran(cand, ws, candoff, 0, tag); *(u32*)(s + L_KEYS + tid * 4) = cand[0]; }
  BAR();
  STAMP(22, 4);
  const bool f1 = loc < 30 && w < 4;
  u32x4 fw[4], xg[4];
  { const u32 a = L_F1W + (w >> 1 & 1) * 8192 + (8 * kq + qp) * 64 + (((w ^ kq) & 1) * 32) + pp * 8;
#pragma unroll
    for (int ks = 0; ks < 4; ++ks) fw[ks] = cat(trrd(s, a + ks * 2048), trrd(s, a + ks * 2048 + 256)); }
  { u32 key; const u32 rk = rank128(s, tid, key); if (rk < 8 && (tid & 3) == 0) *(u32*)(s + L_SEL + rk * 4) = key; }
  BAR();
  STAMP(4, 23);
  const u32 selkey = *(u32*)(s + L_SEL + (lane & 7) * 4), seltok = 2047 - (selkey & 0xffffu);
  rsrc_t rX = mk_rsrc(p.x, 2048 * 1280), rWo = mk_rsrc(p.wo, 640 * 1280);
  if (f1) {
    const u32 vo = seltok * 1280 + kq * 16, so = (loc % 5) * 256;
    ld128<0>(xg[0], rX, vo, so); ld128<64>(xg[1], rX, vo, so); ld128<128>(xg[2], rX, vo, so); ld128<192>(xg[3], rX, vo, so);
  }
  const u32 vwa = (lane >> 3) * 1280 + (((lane & 7) ^ (2 * ((kq & 1) | ((w & 1) << 1)))) * 16);
  const u32 vwb = ((lane >> 1 & 19) | (lane >> 2 & 4) | (lane & 8)) * 1280 + 128 + (lane & 1) * 16;
  const u32 swa = w * 10240 + sl * 160, swb = w * 40960 + sl * 160;
  auto wo_round = [&](auto ic) { constexpr int i = decltype(ic)::v;
    if (i < 7 || w < 4) dma<i * 8192>(rWo, vwa, swa + i * 81920, sb1);
    if ((w >> 1) == (i & 3) && (i < 7 || w == 6)) dma<L_WOB + (i >> 2) * 8192>(rWo, vwb, swb + (i >> 2) * 8 * 40960, sb1);
  };
  wo_round(IC<0>{}); wo_round(IC<1>{}); wo_round(IC<2>{});
  const int c0 = (5 * h) >> 1, krow = 2 * gp - 4 + w;
  const u32 fcol = ((c0 + 2 * (lane & 1)) >> 4) * 128;
  const u32 flq = WS_FG + (2 * gp + (w >> 2)) * 2048 + fcol, flk = WS_FG + krow * 2048 + (lane & 2 ? 1280 : 640) + fcol;
  const u32 fl1 = (lane < 4 && w < 6) ? flk : flq, fl0 = krow < 0 ? flq : fl1;
  u32 flv; ld32_sc1(flv, ws, fl0, 0);
  STAMP(23, 24);
  if (f1) WAITV(9); else if (w < 6) WAITV(5); else WAITV(4);
  arrive_flag(s, 1, ws, ct < 15 ? WS_FG + ((16 + r) * 16 + ct) * 128 : OOB, tag, lane);
  STAMP(24, 5);
  if (f1) {
    WAITV(6);
    f32x4 c = {0.f, 0.f, 0.f, 0.f};
#pragma unroll
    for (int ks = 0; ks < 4; ++ks) { PIN(xg[ks]); c = mfma(fw[ks], xg[ks], c); }
    const u32 po = WS_P1 + (((xcd * 6 + loc / 5) * 5 + loc % 5) * 2 + (w >> 1)) * 1024 + (w & 1) * 512 + kq * 128 + l15 * 16;
    st128_sc1(gran_pack(c, tag), ws, l15 < 8 ? po : OOB);
    WAITV(2);
  } else WAITV(1);
  PIN(flv); spin_flags(flv, ws, fl0, 0, tag);
  STAMP(5, 6);
  const u32 skv = WS_QKV + (80 + c0) * 32768 + krow * 1024;
  const u32 vq = (c0 + (kq > 2 ? 2 : kq)) * 32768 + (gp * 128 + w * 16 + l15) * 16;
  u32x4 q0, q1;
  if (w < 6) attn_stage<L_A0>(ws, sb1, krow < 0 ? OOB : skv, lane);
  ld128_sc1(q0, ws, vq, WS_QKV);
  ld32_sc1(flv, ws, fl1, 16 * 2048);
  STAMP(6, 25);
  WAITV(0); PIN(flv); spin_flags(flv, ws, fl1, 16 * 2048, tag);
  STAMP(25, 26);
  if (w < 6) attn_stage<L_A1>(ws, sb1, skv + 16 * 1024, lane);
  ld128_sc1(q1, ws, vq, WS_QKV + 1024 * 16);
  const int odd = h & 1;
  const u32x2 ones = {0x3f803f80u, 0x3f803f80u};
  const u32 aone = 18432 + (odd ? 0 : 12544 + 8) + w * 1024 + lane * 16;
  if (w < 6 && krow >= 0) *(u32x2*)(s + L_A0 + aone) = ones;
  const bool qlo = kq == 3 || (odd && kq == 0), qhi = kq == 3 || (!odd && kq == 2);
  PIN(q0); q0[0] = qlo ? 0u : q0[0]; q0[1] = qlo ? 0u : q0[1]; q0[2] = qhi ? 0u : q0[2]; q0[3] = qhi ? 0u : q0[3];
  const u32 vrow = L_A0 + 18432 + (16 * w + 4 * kq + qp) * 16;
  u32 aK = L_A0 + (kq > 2 ? 2 : kq) * 6144 + w * 256 + l15 * 16;
  u32 aV0 = vrow + ((pp + odd) >> 1) * 6272 + ((pp + odd) & 1) * 8;
  u32 aV1 = vrow + (pp == 0 ? 12544 + odd * 8 : (odd ? 0 : 12544 + 8));
  PIN(aK); PIN(aV0); PIN(aV1);
  u32 vo0 = (c0 + ((kq + odd) >> 1)) * 32768 + ((kq + odd) & 1) * 8 + l15 * 16, vo1 = kq == 0 ? (c0 + 2) * 32768 + odd * 8 + l15 * 16 : OOB;
  const u32 so = WS_O + (gp * 128 + w * 16) * 16;
  PIN(vo0); PIN(vo1);
  BAR();
  STAMP(26, 7);
  attn_unit(s, q0, aK, aV0, aV1, gp < 2 ? (16 - gp * 8 - w > 0 ? 16 - gp * 8 - w : 0) : 0, lane, ws, vo0, vo1, so,
            [&](auto bc) { constexpr int b = decltype(bc)::v; if constexpr (b < 5) wo_round(IC<b + 3>{}); });
  STAMP(7, 8);
  if (w < 4 || w == 6) WAITV(8); else WAITV(7);
  aK += L_A1 - L_A0; aV0 += L_A1 - L_A0; aV1 += L_A1 - L_A0; PIN(aK); PIN(aV0); PIN(aV1);
  if (w < 6) *(u32x2*)(s + L_A1 + aone) = ones;
  PIN(q1); q1[0] = qlo ? 0u : q1[0]; q1[1] = qlo ? 0u : q1[1]; q1[2] = qhi ? 0u : q1[2]; q1[3] = qhi ? 0u : q1[3];
  BAR();
  STAMP(8, 9);
  const int s64 = xcd * 3 + loc / 10, cgp = loc % 10;
  const bool f2 = loc < 30;
  u32x4 pp1[10];
  const u32 vp1 = WS_P1 + (2 * s64 + (w & 1)) * 10240 + lane * 16;
  auto p1load = [&]() { static_for<10>([&](auto ic) { constexpr int i = decltype(ic)::v; ld128_sc1<(i & 3) * 1024>(pp1[i], ws, vp1, (i >> 2) * 4096); }); };
  const int tokA = 128 * xcd + 32 * oi;
  const u32 fa = WS_FA + (xcd * 32 + (lane & 31)) * 128;
  const int tk = (tid * 6554) >> 16, pc = tid - tk * 10;
  u32x4 of[5], xv0, xv1; u32x2 g0, g1;
  const u32 xo = tid < 320 ? (tokA + tk) * 1280 + sl * 160 + pc * 16 : OOB, go = WS_GG + (tokA + (tk & 31)) * 8;
  attn_unit(s, q1, aK, aV0, aV1, 0, lane, ws, vo0, vo1, so + 1024 * 16, [&](auto bc) { constexpr int b = decltype(bc)::v;
    if constexpr (b == 0) dma<L_A0>(rWo, vwa, swa + 60 * 10240, sb1);
    if constexpr (b == 1) { dma<L_A0 + 8192>(rWo, vwa, swa + 68 * 10240, sb1); if (w < 5) dma<L_A0 + 20480>(rWo, vwb, swb + 15 * 40960, sb1); }
    if constexpr (b == 2) { if (w < 4) dma<L_A0 + 16384>(rWo, vwa, swa + 76 * 10240, sb1); if (w >= 6) p1load(); }
    if constexpr (b == 3) dma<L_F2W>(mk_rsrc(p.wd, 1536 * 1280), vwa, (s64 * 64 + 8 * w) * 1280 + cgp * 128, sb1);
    if constexpr (b == 4) {
      if (w < 4) WAITV(5); else if (w == 4) WAITV(4); else if (w == 5) WAITV(3); else WAITV(13);
      arrive_flag(s, 2, ws, WS_FA + (gp * 32 + h) * 128, tag, lane);
    }
    if constexpr (b == 5) { ld128(xv0, rX, xo, 0); ld128(xv1, rX, xo, 1024 * 1280); ld64_sc1(g0, ws, go, 0); ld64_sc1(g1, ws, go, 8192); }
  });
  STAMP(9, 10);
  WAITV(2);
  STAMP(10, 27);
  if (f2 && w >= 6) {
#pragma unroll 1
    for (int it = 0; it < SPIN_MAX; ++it) {
      bool ok = true;
#pragma unroll
      for (int i = 0; i < 10; ++i) { PIN(pp1[i]); ok = ok && pp1[i][3] == tag; }
      if (__all(ok)) break;
      p1load(); WAITV(0);
    }
    f32x4 g = gran_unpack(pp1[0]), u = gran_unpack(pp1[1]);
#pragma unroll
    for (int i = 1; i < 5; ++i) { g += gran_unpack(pp1[2 * i]); u += gran_unpack(pp1[2 * i + 1]); }
    u32x2 hv = {pk(g[0] * sigmoidf_(g[0]) * u[0], g[1] * sigmoidf_(g[1]) * u[1]), pk(g[2] * sigmoidf_(g[2]) * u[2], g[3] * sigmoidf_(g[3]) * u[3])};
    *(u32x2*)(s + L_H + (lane & 7) * 128 + (w & 1) * 64 + (lane >> 3) * 8) = hv;
  }
  BAR();
  STAMP(27, 28);
  const u32 fsw = ((qp >> 1) | ((kq & 1) << 1)) * 32 + pp * 8;
  if (f1) {
    const u32 a = L_F2W + (8 * kq + qp) * 128 + (fsw ^ (w * 32)), ah = L_H + (l15 & 7) * 128 + kq * 16;
    f32x4 c = {0.f, 0.f, 0.f, 0.f};
    c = mfma(cat(trrd(s, a), trrd(s, a + 512)), lds128(s, ah), c);
    c = mfma(cat(trrd(s, a + 4096), trrd(s, a + 4096 + 512)), lds128(s, ah + 64), c);
    st128_sc1(gran_pack(c, tag), ws, l15 < 8 ? WS_Y + ((s64 * 8 + l15) * 160 + cgp * 16 + w * 4 + kq) * 16 : OOB);
    WAITV(1);
  } else WAITV(0);
  arrive_flag(s, 3, ws, WS_FA + ((8 + gp) * 32 + h) * 128, tag, lane);
  ld32_sc1(flv, ws, fa, 0);
  STAMP(28, 11);
  u32x4 bw[5][5];
  { const int qd = w >> 1;
    const u32 a = (qd < 3 ? qd * 20480 : L_A0) + (8 * kq + qp) * 128;
    u32 ab = (qd < 3 ? L_WOB + qd * 5120 : L_A0 + 20480) + ((kq >> 1) * 16 + (kq & 1) * 4 + qp) * 32 + pp * 8, an[4];
    PIN(ab);
#pragma unroll
    for (int nt = 0; nt < 4; ++nt) { an[nt] = a + (fsw ^ (nt * 32)); PIN(an[nt]); }
#pragma unroll
    for (int ks = 0; ks < 5; ++ks) {
#pragma unroll
      for (int nt = 0; nt < 4; ++nt) bw[ks][nt] = cat(trrd(s, an[nt] + ks * 4096), trrd(s, an[nt] + ks * 4096 + 512));
      bw[ks][4] = cat(trrd(s, ab + ks * 1024), trrd(s, ab + ks * 1024 + 256));
    } }
  WAITV(0);
  PIN(flv); spin_flags(flv, ws, fa, 0, tag);
  STAMP(11, 12);
  const u32 ofo = (w >> 1) * 20 * 32768 + kq * 32768 + (tokA + (w & 1) * 16 + l15) * 16;
#pragma unroll
  for (int ks = 0; ks < 5; ++ks) ld128_sc1(of[ks], ws, ofo, WS_O + ks * 4 * 32768);
  PIN(g0); PIN(g1); PIN(xv0); PIN(xv1);
  spin_gran(g0, ws, go, 0, tag); spin_gran(g1, ws, go, 8192, tag);
  const float ga = __uint_as_float(g0[0]), gb = __uint_as_float(g1[0]);
  float ba[8], bb[8];
#pragma unroll
  for (int e = 0; e < 4; ++e) {
    ba[2 * e] = (1.f - ga) * blo(xv0[e]); ba[2 * e + 1] = (1.f - ga) * bhi(xv0[e]);
    bb[2 * e] = (1.f - gb) * blo(xv1[e]); bb[2 * e + 1] = (1.f - gb) * bhi(xv1[e]);
  }
  u32 ared = w * 5440 + (lane >> 5) * 544 + (lane & 31) * 16, afin = (tk >> 4) * 5440 + (pc >> 1) * 1088 + (pc & 1) * 544 + (tk & 15) * 16;
  u32 ared1 = ared + 43520, afin1 = afin + 43520;
  PIN(ared); PIN(afin); PIN(ared1); PIN(afin1);
  rsrc_t rO = mk_rsrc(p.out, 2048 * 1280);
  BAR();
  WAITV(0);
#pragma unroll
  for (int ks = 0; ks < 5; ++ks) PIN(of[ks]);
  ld32_sc1(flv, ws, fa, 256 * 128);
  o_compute(s, ared, bw, of);
  STAMP(12, 13);
  for (u32 m = (u32)__ballot(lane < 8 && ((seltok & 1023u) - (u32)tokA) < 32u); m; m &= m - 1) {
    const int c = __builtin_ctz(m);
    const u32 key = __builtin_amdgcn_readlane(selkey, c), tok = 2047 - (key & 0xffffu);
    const int sp = (tid * 3277) >> 16, c4 = tid - sp * 20;
    const u32 yo = WS_Y + (((sp < 24 ? sp : 0) * 8 + c) * 160 + sl * 20 + c4) * 16;
    u32x4 y;
#pragma unroll 1
    for (int it = 0; it < SPIN_MAX; ++it) { ld128_sc1(y, ws, yo, 0); WAITV(0); PIN(y); if (__all(y[3] == tag)) break; }
    *(f32x4*)(s + L_A1 + tid * 16) = gran_unpack(y);
    BAR();
    if (tid < 80) { float t = 0.f;
#pragma unroll
      for (int q = 0; q < 24; ++q) t += *(float*)(s + L_A1 + q * 320 + tid * 4);
      *(float*)(s + L_YS + tid * 4) = t; }
    BAR();
    if ((u32)(tokA + tk) == (tok & 1023u)) {
      const u32 o = key >> 16, rb = (o & 0x8000u) ? (o ^ 0x8000u) : (o ^ 0xffffu);
      const float rs = sigmoidf_(__uint_as_float(rb << 16));
      const f32x4 ya = *(f32x4*)(s + L_YS + pc * 32), yb = *(f32x4*)(s + L_YS + pc * 32 + 16);
      const float fa_ = tok < 1024 ? (1.f - ga) * rs : 0.f, fb_ = tok < 1024 ? 0.f : (1.f - gb) * rs;
#pragma unroll
      for (int e = 0; e < 4; ++e) { ba[e] += fa_ * ya[e]; ba[4 + e] += fa_ * yb[e]; bb[e] += fb_ * ya[e]; bb[4 + e] += fb_ * yb[e]; }
    }
  }
  BAR();
  if (b == 0 && w == 7) {
    u32 f[4];
#pragma unroll
    for (int i = 0; i < 4; ++i) ld32_sc1(f[i], ws, (lane & 15) < 15 ? WS_FG + (i * 64 + lane) * 128 : WS_CAND + (i * 4 + kq) * 128 + 4, 0);
    WAITV(0);
#pragma unroll
    for (int i = 0; i < 4; ++i) { PIN(f[i]); spin_flags(f[i], ws, (lane & 15) < 15 ? WS_FG + (i * 64 + lane) * 128 : WS_CAND + (i * 4 + kq) * 128 + 4, 0, tag); }
    if (lane == 0) st32_sc1(tag, ws, WS_EP);
  }
  WAITV(0); PIN(flv);
  const bool rdy = __all(flv == tag);
  if (!rdy) { o_final(s, afin, ga, ba, rO, xo, 0); spin_flags(flv, ws, fa, 256 * 128, tag); }
  STAMP(13, 14);
#pragma unroll
  for (int ks = 0; ks < 5; ++ks) ld128_sc1(of[ks], ws, ofo, WS_O + ks * 4 * 32768 + 1024 * 16);
  if (rdy) { o_final(s, afin, ga, ba, rO, xo, 0); WAITV(1); } else WAITV(0);
#pragma unroll
  for (int ks = 0; ks < 5; ++ks) PIN(of[ks]);
  STAMP(14, 15);
  o_compute(s, ared1, bw, of);
  BAR();
  STAMP(15, 16);
  if (w < 5) o_final(s, afin1, gb, bb, rO, xo, 1024 * 1280);
  STAMP(16, 17);
  asm volatile("fk_code_hi:" ::: "memory");
}

__global__ __launch_bounds__(512, 2) void probe(Params p) {  if (p.ws == nullptr) *(u32*)p.out = 0; }
extern "C" void kernel_launch(void* const* d_in, const int* in_sizes, int n_in, void* d_out, int out_size, void* d_ws, size_t ws_size,
                              hipStream_t stream) {
  Params p;
  p.x = (const u16*)d_in[0]; p.wqkv = (const u16*)d_in[1]; p.wo = (const u16*)d_in[2]; p.wr = (const u16*)d_in[4];
  p.wg = (const u16*)d_in[5]; p.wu = (const u16*)d_in[6]; p.wd = (const u16*)d_in[7]; p.gw = (const u16*)d_in[8];
  p.out = (u16*)d_out; p.ws = (char*)d_ws;
  (void)hipFuncSetAttribute((const void*)fused_kernel, hipFuncAttributeMaxDynamicSharedMemorySize, LDS_BYTES);
  fused_kernel<<<256, 512, LDS_BYTES, stream>>>(p);
  probe<<<256, 512, 0, stream>>>(p);
}
```

```cpp
#include <hip/hip_runtime.h>
#include <hip/hip_bf16.h>

typedef unsigned short u16;
typedef unsigned int u32;
typedef unsigned long long u64;
typedef __attribute__((ext_vector_type(2))) u32 u32x2;
typedef __attribute__((ext_vector_type(4))) u32 u32x4;
typedef __attribute__((ext_vector_type(4))) float f32x4;
typedef __attribute__((ext_vector_type(8))) __bf16 bf16x8;
typedef __attribute__((ext_vector_type(2))) __bf16 bf16x2;
typedef __attribute__((ext_vector_type(4))) short s16x4;
#define LDSP(T) __attribute__((address_space(3))) T*
typedef __amdgpu_buffer_rsrc_t rsrc_t;
#define DEV __device__ __forceinline__

constexpr u32 WS_QKV = 0;
constexpr u32 WS_O   = WS_QKV + 240u * 32768;
constexpr u32 WS_P1  = WS_O + 80u * 32768;
constexpr u32 WS_Y   = WS_P1 + 480u * 1024;
constexpr u32 WS_FG  = WS_Y + 24u * 8 * 160 * 16;
constexpr u32 WS_FA  = WS_FG + 512u * 128;
constexpr u32 WS_CAND = WS_FA + 512u * 128;
constexpr u32 WS_SEL = WS_CAND + 16u * 128;
constexpr u32 WS_GG  = WS_SEL + 16u * 128;
constexpr u32 WS_EP  = WS_GG + 2048u * 8;
constexpr u32 WS_END = WS_EP + 128;
constexpr u32 WS_STAMP = WS_END;

constexpr u32 L_X1   = 73728;
constexpr u32 L_F1W  = 40960;
constexpr u32 L_A0   = 76800;
constexpr u32 L_A1   = 114048;
constexpr u32 L_F2W  = 151296;
constexpr u32 L_RW   = 155648;
constexpr u32 L_CNT  = 159488;
constexpr u32 L_H    = 159744;
constexpr u32 L_KEYS = 160768;
constexpr u32 L_RB   = 161280;
constexpr u32 L_Y    = 87040;
constexpr u32 LDS_BYTES = 163840;
constexpr int SPIN_MAX = 1 << 14;
constexpr u32 OOB = 0x80000000u;

#define STAMP(prev, i) do {} while (0)
#define STAMP_ARG
#define STAMP_PASS

DEV rsrc_t mk_rsrc(u64 p, u32 bytes) { return __builtin_amdgcn_make_buffer_rsrc((void*)p, 0, bytes, 0x00020000); }
DEV u32 rfl(u32 v) { return (u32)__builtin_amdgcn_readfirstlane((int)v); }
template <u32 L> DEV void dma(rsrc_t r, u32 voff, u32 soff, u32 lds) {
  asm volatile("s_add_u32 m0, %0, %4\n\ts_nop 0\n\tbuffer_load_dwordx4 %1, %2, %3 offen lds" :: "s"(lds), "v"(voff), "s"(r), "s"(soff), "n"(L) : "memory", "scc");
}
template <u32 L> DEV void dma_sc1(rsrc_t r, u32 voff, u32 soff, u32 lds) {
  asm volatile("s_add_u32 m0, %0, %4\n\ts_nop 0\n\tbuffer_load_dwordx4 %1, %2, %3 offen sc1 lds" :: "s"(lds), "v"(voff), "s"(r), "s"(soff), "n"(L) : "memory", "scc");
}
template <u32 L> DEV void dma3(rsrc_t rX, rsrc_t rW, u32 vX, u32 vW0, u32 vW1, u32 sX, u32 sW, u32 lds) {
  asm volatile("s_add_u32 m0, %0, %8\n\ts_nop 0\n\tbuffer_load_dwordx4 %1, %2, %3 offen lds\n\t"
               "buffer_load_dwordx4 %4, %6, %7 offen offset:1024 lds\n\tbuffer_load_dwordx4 %5, %6, %7 offen offset:2048 lds"
               :: "s"(lds), "v"(vX), "s"(rX), "s"(sX), "v"(vW0), "v"(vW1), "s"(rW), "s"(sW), "n"(L) : "memory", "scc");
}
template <u32 I> DEV void dma_cont(rsrc_t r, u32 voff, u32 soff) {
  asm volatile("buffer_load_dwordx4 %0, %1, %2 offen offset:%3 lds" :: "v"(voff), "s"(r), "s"(soff), "n"(I) : "memory");
}
template <u32 I = 0> DEV void ld128_sc1(u32x4& d, rsrc_t r, u32 voff, u32 soff) { asm volatile("buffer_load_dwordx4 %0, %1, %2, %3 offen offset:%4 sc1" : "=&v"(d) : "v"(voff), "s"(r), "s"(soff), "n"(I) : "memory"); }
template <u32 I = 0> DEV void ld128(u32x4& d, rsrc_t r, u32 voff, u32 soff) { asm volatile("buffer_load_dwordx4 %0, %1, %2, %3 offen offset:%4" : "=&v"(d) : "v"(voff), "s"(r), "s"(soff), "n"(I) : "memory"); }
template <u32 I> DEV void gld128(u32x4& d, const char* a) { asm volatile("global_load_dwordx4 %0, %1, off offset:%2" : "=&v"(d) : "v"(a), "n"(I) : "memory"); }
DEV void ld64_sc1(u32x2& d, rsrc_t r, u32 voff, u32 soff) { asm volatile("buffer_load_dwordx2 %0, %1, %2, %3 offen sc1" : "=&v"(d) : "v"(voff), "s"(r), "s"(soff) : "memory"); }
DEV void ld32_sc1(u32& d, rsrc_t r, u32 voff, u32 soff) { asm volatile("buffer_load_dword %0, %1, %2, %3 offen sc1" : "=&v"(d) : "v"(voff), "s"(r), "s"(soff) : "memory"); }
#define WAITV(n) asm volatile("s_waitcnt vmcnt(%0)" :: "n"(n) : "memory")
template <class T> DEV void pin_(T& x) { asm volatile("" : "+v"(x)); }
#define PIN(x) pin_(x)
#define BAR() do { asm volatile("" ::: "memory"); __builtin_amdgcn_s_waitcnt(0xC07F); asm volatile("s_barrier" ::: "memory"); } while (0)
#define SCHED() __builtin_amdgcn_sched_barrier(0)
DEV void st64_sc1(u32x2 v, rsrc_t r, u32 voff, u32 soff = 0) { __builtin_amdgcn_raw_buffer_store_b64(v, r, voff, soff, 16); }
DEV void st128_sc1(u32x4 v, rsrc_t r, u32 voff) { __builtin_amdgcn_raw_buffer_store_b128(v, r, voff, 0, 16); }
DEV void st32_sc1(u32 v, rsrc_t r, u32 voff) { __builtin_amdgcn_raw_buffer_store_b32(v, r, voff, 0, 16); }

DEV u32 pk(float a, float b) { bf16x2 v = {(__bf16)a, (__bf16)b}; return __builtin_bit_cast(u32, v); }
DEV float rbf(float a) { return (float)(__bf16)a; }
DEV float blo(u32 v) { return __uint_as_float(v << 16); }
DEV float bhi(u32 v) { return __uint_as_float(v & 0xffff0000u); }
DEV f32x4 mfma(u32x4 a, u32x4 b, f32x4 c) { return __builtin_amdgcn_mfma_f32_16x16x32_bf16(__builtin_bit_cast(bf16x8, a), __builtin_bit_cast(bf16x8, b), c, 0, 0, 0); }
DEV u32x2 trrd(char* s, u32 a) { return __builtin_bit_cast(u32x2, __builtin_amdgcn_ds_read_tr16_b64_v4i16((LDSP(s16x4))(s + a))); }
DEV u32x4 lds128(char* s, u32 a) { return *(u32x4*)(s + a); }
DEV u32x4 cat(u32x2 a, u32x2 b) { u32x4 r = {a[0], a[1], b[0], b[1]}; return r; }
DEV float ex2(float v) { return __builtin_amdgcn_exp2f(v); }
DEV float sigmoidf_(float v) { return __builtin_amdgcn_rcpf(1.f + ex2(v * -1.4426950408889634f)); }
DEV u32x4 gran_pack(f32x4 c, u32 tag) {
  const u32 a = __float_as_uint(c[0]), b = __float_as_uint(c[1]), d = __float_as_uint(c[2]), e = __float_as_uint(c[3]);
  u32x4 r = {__builtin_amdgcn_perm(a, e, 0x07060503u), __builtin_amdgcn_perm(b, e, 0x07060502u), __builtin_amdgcn_perm(d, e, 0x07060501u), tag};
  return r;
}
DEV f32x4 gran_unpack(u32x4 g) {
  const u32 t = __builtin_amdgcn_perm(g[0], g[1], 0x04000c0cu);
  f32x4 r = {__uint_as_float(g[0]), __uint_as_float(g[1]), __uint_as_float(g[2]), __uint_as_float(__builtin_amdgcn_perm(t, g[2], 0x0706000cu))};
  return r;
}

DEV void arrive_flag(char* s, int cnt, rsrc_t ws, u32 flagoff, u32 tag, int lane) {
  if (lane == 0) {
    u32 old;
    asm volatile("ds_add_rtn_u32 %0, %1, %2\n\ts_waitcnt lgkmcnt(0)" : "=v"(old) : "v"((u32)(size_t)(LDSP(char))(s + L_CNT + cnt * 4)), "v"(1u) : "memory");
    st32_sc1(tag, ws, old == 7 ? flagoff : OOB);
  }
}
DEV void spin_flags(u32 v, rsrc_t ws, u32 voff, u32 soff, u32 tag) {
#pragma unroll 1
  for (int i = 0; i < SPIN_MAX && !__all(v == tag); ++i) { ld32_sc1(v, ws, voff, soff); WAITV(0); PIN(v); }
}
DEV void spin_gran(u32x2& v, rsrc_t ws, u32 voff, u32 soff, u32 tag) {
#pragma unroll 1
  for (int i = 0; i < SPIN_MAX && !__all(v[1] == tag); ++i) { ld64_sc1(v, ws, voff, soff); WAITV(0); PIN(v); }
}
DEV u32 rank128(char* s, int tid, u32& key) {
  key = *(u32*)(s + L_KEYS + (tid >> 2) * 4);
  u32 c = 0;
#pragma unroll
  for (int i = 0; i < 8; ++i) {
    u32x4 k = lds128(s, L_KEYS + (tid & 3) * 128 + i * 16);
    c += (k[0] > key) + (k[1] > key) + (k[2] > key) + (k[3] > key);
  }
  c += __builtin_amdgcn_update_dpp(0u, c, 0xB1, 0xf, 0xf, true);
  c += __builtin_amdgcn_update_dpp(0u, c, 0x4E, 0xf, 0xf, true);
  return c;
}

DEV void g_epilogue(f32x4 (&acc)[2][2], rsrc_t ws, u32 voff, u32 soff, bool isq) {
  if (isq) {
#pragma unroll
    for (int i = 0; i < 4; ++i) acc[i >> 1][i & 1] *= 0.32259863752095353f;
  }
#pragma unroll
  for (int ft = 0; ft < 2; ++ft)
#pragma unroll
    for (int tt = 0; tt < 2; ++tt) {
      f32x4 a = acc[ft][tt];
      u32x2 v = {pk(a[0], a[1]), pk(a[2], a[3])};
      st64_sc1(v, ws, voff + tt * 256, soff + ft * 65536);
    }
}
constexpr u32 ring_slot(int i) { return (i & 3) == 3 ? L_X1 + 7 * 8192 : (i & 3) * 24576; }
template <int I> struct IC { static constexpr int v = I; };
template <int N, class F> DEV void static_for(F&& f) { if constexpr (N > 0) { static_for<N - 1>(f); f(IC<N - 1>{}); } }

template <class Pre>
DEV void g_phase(rsrc_t rX, u64 pw, char* s, u32 sb, rsrc_t ws, int w, int lane, int r, int ct, int rot,
                 u32& tag, u32 epv, u32 pf, u32& flv, u32 fl0, Pre&& pre STAMP_ARG) {
  const int cg = w & 3, th = w >> 2, kq = lane >> 4, l15 = lane & 15;
  rsrc_t rW = mk_rsrc(pw - 2048, 640 * 3840 + 2048);
  const u32 vW = kq * 3840 + ((l15 ^ (2 * (kq | ((w & 1) << 2)))) * 16), vW0 = vW + 1024, vW1 = vW + 4 * 3840;
  const u32 vX = (lane >> 3) * 1280 + (((lane & 7) ^ ((4 * w + kq) & 7)) * 16);
  const u32 sW = 8 * w * 3840 + ct * 256, sX = (64 * r + 8 * w) * 1280, sb3 = sb + w * 3072, sb1 = sb + w * 1024;
  u32 k0 = rot * 128, k1 = rot * 128;
  auto batch = [&](auto ic) { constexpr int i = decltype(ic)::v;
    dma3<ring_slot(i)>(rX, rW, vX, vW0, vW1, sX + k0, sW + k0 * 1920, sb3); k0 = k0 == 1152 ? 0 : k0 + 128; };
  auto x1 = [&](auto jc) { constexpr int j = decltype(jc)::v;
    dma<L_X1 + j * 8192>(rX, vX, sX + k1 + 1024 * 1280, sb1); k1 = k1 == 1152 ? 0 : k1 + 128; };
  STAMP(0, 18);
  batch(IC<0>{});
  asm volatile("s_barrier" ::: "memory");
  batch(IC<1>{});
  asm volatile("s_barrier" ::: "memory");
  batch(IC<2>{});
  STAMP(18, 19);
  const int qp = (lane >> 2) & 3, pp = lane & 3, hrow = qp | ((kq & 1) << 2);
  u32 aW0 = kq * 3072 + 1024 + qp * 256 + (((cg * 4 + (pp >> 1)) ^ (2 * hrow)) * 16) + (pp & 1) * 8, aW1 = aW0 ^ 32;
  const u32 xsw = (kq ^ (l15 >> 1)) * 16;
  u32 aR0 = (4 * th + (l15 >> 3)) * 3072 + (l15 & 7) * 128 + xsw, aR1 = aR0 ^ 64;
  u32 aS0 = L_X1 + (32 * th + l15) * 128 + xsw, aS1 = aS0 ^ 64;
  PIN(aW0); PIN(aW1); PIN(aR0); PIN(aR1); PIN(aS0); PIN(aS1);
  u32x4 wf[20][2];
  f32x4 acc[2][2] = {}, acd[2][2] = {};
  u32x4 xf[2][4], xh[10][4];
  auto mm0 = [&](auto ic, int h) { constexpr int i = decltype(ic)::v;
#pragma unroll
    for (int q = 0; q < 4; ++q) acc[q & 1][q >> 1] = mfma(wf[2 * i + h][q & 1], xf[i & 1][2 * h + (q >> 1)], acc[q & 1][q >> 1]); };
  auto rd1 = [&](int j) {
#pragma unroll
    for (int q = 0; q < 4; ++q) xh[j][q] = lds128(s, ((q >> 1) ? aS1 : aS0) + (j < 6 ? j : j - 6) * 8192 + (q & 1) * 2048); };
  auto mm1 = [&](int j) {
#pragma unroll
    for (int q = 0; q < 8; ++q) acd[q & 1][q >> 1 & 1] = mfma(wf[2 * j + (q >> 2)][q & 1], xh[j][q >> 1], acd[q & 1][q >> 1 & 1]); };
  static_for<10>([&](auto ic) { constexpr int i = decltype(ic)::v;
    constexpr u32 base = (i & 3) == 3 ? 0 : ring_slot(i), jump = (i & 3) == 3 ? ring_slot(3) : (i & 3) == 0 && i ? -ring_slot(3) : 0;
    if constexpr (jump != 0) { aW0 += jump; aW1 += jump; aR0 += jump; aR1 += jump; PIN(aW0); PIN(aW1); PIN(aR0); PIN(aR1); }
    const u32 kx = sX + k0, kw = sW + k0 * 1920;
    k0 = k0 == 1152 ? 0 : k0 + 128;
    WAITV(i < 8 ? 6 : i == 8 ? 6 : 7);
    BAR();
    if constexpr (i < 7) dma<ring_slot(i + 3)>(rX, vX, kx, sb3); else x1(IC<i == 7 ? 0 : i == 8 ? 3 : 7>{});
    if (i == 0) { PIN(epv); PIN(pf); tag = rfl(epv) * 1664525u + 1013904223u; STAMP(19, 20); }
#pragma unroll
    for (int ks = 0; ks < 2; ++ks) {
      wf[2 * i + ks][0] = cat(trrd(s, base + aW0 + ks * 12288), trrd(s, base + aW0 + ks * 12288 + 1024));
      xf[i & 1][2 * ks] = lds128(s, base + (ks ? aR1 : aR0));
      wf[2 * i + ks][1] = cat(trrd(s, base + aW1 + ks * 12288), trrd(s, base + aW1 + ks * 12288 + 1024));
      xf[i & 1][2 * ks + 1] = lds128(s, base + (ks ? aR1 : aR0) + 6144);
    }
    SCHED();
    if constexpr (i > 0) mm0(IC<i - 1>{}, 0);
    SCHED();
    if constexpr (i < 7) dma_cont<1024>(rW, vW0, kw); else x1(IC<i == 7 ? 1 : i == 8 ? 4 : 8>{});
    SCHED();
    if constexpr (i > 0) mm0(IC<i - 1>{}, 1);
    SCHED();
    if constexpr (i < 7) dma_cont<2048>(rW, vW1, kw); else x1(IC<i == 7 ? 2 : i == 8 ? 5 : 9>{});
    if constexpr (i == 8) x1(IC<6>{});
  });
  mm0(IC<9>{}, 0); mm0(IC<9>{}, 1);
  STAMP(20, 1);
  const u32 vst = (kq >> 1) * 32768 + l15 * 16 + (kq & 1) * 8, sst = WS_QKV + (ct * 16 + cg * 4) * 32768 + (64 * r + 32 * th) * 16;
  g_epilogue(acc, ws, vst, sst, ct < 5);
  WAITV(8);
  BAR();
  STAMP(1, 2);
  rd1(0); rd1(1); pre(IC<0>{}); SCHED();
  rd1(2); pre(IC<1>{}); mm1(0); SCHED();
  rd1(3); pre(IC<2>{}); mm1(1); SCHED();
  rd1(4); pre(IC<3>{}); mm1(2); SCHED();
  rd1(5); pre(IC<4>{}); mm1(3); SCHED();
  aS0 += 6 * 8192; aS1 += 6 * 8192; PIN(aS0); PIN(aS1);
  mm1(4); SCHED();
  WAITV(5);
  arrive_flag(s, 0, ws, WS_FG + (r * 16 + ct) * 128, tag, lane);
  BAR();
  STAMP(2, 21);
  rd1(6); rd1(7); mm1(5); SCHED();
  rd1(8); mm1(6); SCHED();
  rd1(9); mm1(7); SCHED();
  ld32_sc1(flv, ws, fl0, 0);
  mm1(8); mm1(9);
  STAMP(21, 3);
  g_epilogue(acd, ws, vst, sst + 1024 * 16, ct < 5);
  WAITV(4);
}

template <class Pre>
DEV void r_phase(rsrc_t rX, u64 pwr, u64 pgw, char* s, u32 sb, rsrc_t ws, int w, int lane, int tid, int r,
                 u32& tag, u32 epv, u32 pf, u32& flv, u32 fl0, u32x2& gr, u32& ckey, Pre&& pre STAMP_ARG) {
  const int kq = lane >> 4, l15 = lane & 15;
  const u32 sb1 = sb + w * 1024;
  if ((w & 1) * 1024 + lane * 16 < 1280) {
    if (w < 2) dma<L_RW>(mk_rsrc(pwr, 1280), w * 1024 + lane * 16, 0, sb1);
    else if (w < 4) dma<L_RW + 1536 - 2048>(mk_rsrc(pgw, 1280), (w - 2) * 1024 + lane * 16, 0, sb1);
  }
  u32x4 xr[18], wf[20];
  const u32 v0 = (lane >> 3) * 1280 + (((lane & 7) ^ kq) * 16), v1 = (v0 ^ 64) + 8 * 1280 - 1024, sX = ((w >> 2) * 1024 + 64 * r + 16 * (w & 3)) * 1280;
  static_for<9>([&](auto ic) { constexpr int i = decltype(ic)::v; dma<i * 2048>(rX, v0, sX + i * 128, sb + w * 18432); dma_cont<1024>(rX, v1, sX + i * 128); });
  u32 aw = L_RW + (l15 == 1 ? 1536 : 0) + kq * 16;
  u32 a0 = w * 18432 + l15 * 128 + ((kq ^ (l15 >> 1)) * 16), a1 = a0 ^ 64;
  PIN(aw); PIN(a0); PIN(a1);
  WAITV(18); BAR();
  PIN(epv); PIN(pf); tag = rfl(epv) * 1664525u + 1013904223u;
#pragma unroll
  for (int i = 0; i < 20; ++i) wf[i] = lds128(s, aw + i * 64);
  f32x4 acc = {0.f, 0.f, 0.f, 0.f}, acc2 = acc;
  static_for<3>([&](auto jc) { constexpr int j = decltype(jc)::v;
    SCHED(); WAITV(j == 0 ? 12 : 14 - 6 * j);
#pragma unroll
    for (int i = 3 * j; i < 3 * j + 3; ++i) { xr[2 * i] = lds128(s, a0 + i * 2048); xr[2 * i + 1] = lds128(s, a1 + i * 2048); }
#pragma unroll
    for (int i = 3 * j; i < 3 * j + 3; ++i) { acc = mfma(xr[2 * i], wf[2 * i], acc); acc2 = mfma(xr[2 * i + 1], wf[2 * i + 1], acc2); }
    if (j == 0) { SCHED(); __builtin_amdgcn_s_waitcnt(0xC07F); dma<0>(rX, v0, sX + 1152, sb + w * 18432); dma_cont<1024>(rX, v1, sX + 1152); }
  });
  SCHED(); WAITV(0);
  STAMP(0, 29);
  acc = mfma(lds128(s, a0), wf[18], acc); acc2 = mfma(lds128(s, a1), wf[19], acc2);
  acc += acc2;
  if (l15 < 2) *(f32x4*)(s + L_RB + l15 * 512 + (16 * w + 4 * kq) * 4) = acc;
  BAR();
  STAMP(29, 30);
  if (tid < 128) {
    const int tok = (tid >> 6) * 1024 + 64 * r + (tid & 63);
    const float rv = *(float*)(s + L_RB + tid * 4), gl = *(float*)(s + L_RB + 512 + tid * 4);
    const u32 rb = pk(rv, 0.f) & 0xffffu;
    *(u32*)(s + L_KEYS + tid * 4) = ((rb ^ ((rb & 0x8000u) ? 0xffffu : 0x8000u)) << 16) | (u32)(2047 - tok);
    gr[0] = __float_as_uint(sigmoidf_(gl)); gr[1] = tag;
  }
  BAR();
  u32 key; const u32 rk = rank128(s, tid, key);
  if (rk < 8 && (tid & 3) == 0) { u32x2 c = {key, tag}; st64_sc1(c, ws, WS_CAND + r * 128 + rk * 8); }
  STAMP(30, 3);
  static_for<5>(pre);
  const u32 candoff = WS_CAND + (tid >> 3) * 128 + (tid & 7) * 8;
  if (w < 2) { u32x2 cand; ld64_sc1(cand, ws, candoff, 0); WAITV(0); PIN(cand); spin_gran(cand, ws, candoff, 0, tag); ckey = cand[0]; }
  ld32_sc1(flv, ws, fl0, 0);
  WAITV(0);
}
DEV void r_select(char* s, rsrc_t ws, int w, int tid, int r, u32 tag, u32x2 gr, u32 ckey) {
  BAR();
  if (w < 2) *(u32*)(s + L_KEYS + tid * 4) = ckey;
  BAR();
  u32 key; const u32 rk = rank128(s, tid, key);
  const u32x2 c = {key, tag};
  st64_sc1(c, ws, rk < 8 && (tid & 3) == 0 ? WS_SEL + r * 128 + rk * 8 : OOB);
  st64_sc1(gr, ws, tid < 128 ? WS_GG + ((tid >> 6) * 1024 + 64 * r + (tid & 63)) * 8 : OOB);
}

template <u32 BUF> DEV void attn_stage(rsrc_t ws, u32 sb1, u32 so, int lane) {
  dma_sc1<BUF>(ws, lane * 16, so, sb1);
  dma_sc1<BUF + 6144>(ws, lane * 16, so + 32768, sb1);
  dma_sc1<BUF + 12288>(ws, lane * 16, so + 65536, sb1);
  dma_sc1<BUF + 18432>(ws, lane * 16, so + 80 * 32768, sb1);
  dma_sc1<BUF + 18432 + 6272>(ws, lane * 16, so + 81 * 32768, sb1);
  dma_sc1<BUF + 18432 + 12544>(ws, lane * 16, so + 82 * 32768, sb1);
}
DEV float xmax(float m) {
  auto a = __builtin_amdgcn_permlane16_swap(__float_as_uint(m), __float_as_uint(m), false, false);
  m = fmaxf(__uint_as_float(a[0]), __uint_as_float(a[1]));
  auto b = __builtin_amdgcn_permlane32_swap(__float_as_uint(m), __float_as_uint(m), false, false);
  return fmaxf(__uint_as_float(b[0]), __uint_as_float(b[1]));
}
template <class Mid>
DEV void attn_unit(char* s, u32x4 qf, u32 aK, u32 aV0, u32 aV1, int nskip, int lane, rsrc_t ws, u32 vo0, u32 vo1, u32 so, Mid&& mid) {
  const int kq = lane >> 4, l15 = lane & 15, dg = l15 - 4 * kq;
  const float NEG = -__builtin_inff();
  const f32x4 z = {0.f, 0.f, 0.f, 0.f};
  const u32x2 zz = {0u, 0u};
  u32x4 kf[18], va[9], vb[9];
  kf[16] = lds128(s, aK + 16 * 256); kf[0] = lds128(s, aK); kf[1] = lds128(s, aK + 256);
  va[8] = cat(trrd(s, aV0 + 16 * 256), zz); vb[8] = cat(trrd(s, aV1 + 16 * 256), zz);
  kf[2] = lds128(s, aK + 512); kf[3] = lds128(s, aK + 768);
  va[0] = cat(trrd(s, aV0), trrd(s, aV0 + 256)); vb[0] = cat(trrd(s, aV1), trrd(s, aV1 + 256));
  SCHED();
  f32x4 sd = mfma(kf[16], qf, z);
#pragma unroll
  for (int e = 0; e < 4; ++e) sd[e] = e <= dg ? sd[e] : NEG;
  const float m = xmax(fmaxf(fmaxf(sd[0], sd[1]), fmaxf(sd[2], sd[3])));
  const f32x4 c = {-m, -m, -m, -m};
  f32x4 sa = mfma(kf[0], qf, c), sc = mfma(kf[1], qf, c);
  u32x4 P = {pk(ex2(sd[0] - m), ex2(sd[1] - m)), pk(ex2(sd[2] - m), ex2(sd[3] - m)), 0u, 0u};
  f32x4 o0 = mfma(va[8], P, z), o1 = mfma(vb[8], P, z);
#pragma unroll
  for (int e = 0; e < 4; ++e) sa[e] = e > dg ? sa[e] : NEG;
  static_for<8>([&](auto bc) { constexpr int b = decltype(bc)::v;
    SCHED();
    f32x4 na, nc;
    if (b < 7) na = mfma(kf[2 * b + 2], qf, c);
    if (b < 6) { kf[2 * b + 4] = lds128(s, aK + (2 * b + 4) * 256); kf[2 * b + 5] = lds128(s, aK + (2 * b + 5) * 256); }
    P[0] = pk(ex2(sa[0]), ex2(sa[1])); P[1] = pk(ex2(sa[2]), ex2(sa[3]));
    SCHED();
    if (b < 7) nc = mfma(kf[2 * b + 3], qf, c);
    if (b < 7) {
      va[b + 1] = cat(trrd(s, aV0 + (2 * b + 2) * 256), trrd(s, aV0 + (2 * b + 3) * 256));
      vb[b + 1] = cat(trrd(s, aV1 + (2 * b + 2) * 256), trrd(s, aV1 + (2 * b + 3) * 256));
    }
    P[2] = pk(ex2(sc[0]), ex2(sc[1])); P[3] = pk(ex2(sc[2]), ex2(sc[3]));
    SCHED();
    o0 = mfma(va[b], P, o0);
    o1 = mfma(vb[b], P, o1);
    mid(bc);
    sa = na; sc = nc;
  });
  SCHED();
  auto sw = __builtin_amdgcn_permlane16_swap(__float_as_uint(o1[0]), __float_as_uint(o1[0]), false, false);
  float l = kq == 0 ? __uint_as_float(sw[1]) : o1[0];
  {
    const float big = fmaxf(fmaxf(fmaxf(fabsf(o0[0]), fabsf(o0[1])), fmaxf(fabsf(o0[2]), fabsf(o0[3]))),
                            fmaxf(fmaxf(fabsf(o1[0]), fabsf(o1[1])), fmaxf(fabsf(o1[2]), fabsf(o1[3]))));
    const float sum = (o0[0] + o0[1]) + (o0[2] + o0[3]) + (o1[0] + o1[1]) + (o1[2] + o1[3]);
    const bool ok = l < 1.8446744073709552e19f && big < 1e37f && sum == sum;
    if (__builtin_expect(!__all(ok), 0)) {
      float mt = m;
#pragma unroll 1
      for (int t = nskip; t < 16; ++t) {
        f32x4 v = mfma(lds128(s, aK + t * 256), qf, z);
#pragma unroll
        for (int e = 0; e < 4; ++e) mt = fmaxf(mt, (t > 0 || e > dg) ? v[e] : NEG);
      }
      mt = xmax(mt);
      o0 = z; o1 = z;
#pragma unroll 1
      for (int t = nskip; t < 17; ++t) {
        f32x4 v = mfma(lds128(s, aK + t * 256), qf, z);
#pragma unroll
        for (int e = 0; e < 4; ++e) v[e] = (t == 0 ? e > dg : (t < 16 || e <= dg)) ? ex2(v[e] - mt) : 0.f;
        const u32x4 Q = {pk(v[0], v[1]), pk(v[2], v[3]), 0u, 0u};
        o0 = mfma(cat(trrd(s, aV0 + t * 256), zz), Q, o0);
        o1 = mfma(cat(trrd(s, aV1 + t * 256), zz), Q, o1);
      }
      auto s2 = __builtin_amdgcn_permlane16_swap(__float_as_uint(o1[0]), __float_as_uint(o1[0]), false, false);
      l = kq == 0 ? __uint_as_float(s2[1]) : o1[0];
    }
  }
  const float inv = __builtin_amdgcn_rcpf(l);
  u32x2 v0 = {pk(o0[0] * inv, o0[1] * inv), pk(o0[2] * inv, o0[3] * inv)};
  u32x2 v1 = {pk(o1[0] * inv, o1[1] * inv), pk(o1[2] * inv, o1[3] * inv)};
  st64_sc1(v0, ws, vo0, so);
  st64_sc1(v1, ws, vo1, so);
}

DEV void o_compute(char* s, u32 a, u32x4 (&bw)[5][5], u32x4 (&of)[5]) {
  f32x4 acc[5] = {};
#pragma unroll
  for (int ks = 0; ks < 5; ++ks)
#pragma unroll
    for (int nt = 0; nt < 5; ++nt) acc[nt] = mfma(bw[ks][nt], of[ks], acc[nt]);
#pragma unroll
  for (int nt = 0; nt < 5; ++nt) *(f32x4*)(s + a + nt * 1088) = acc[nt];
}
DEV void o_final(char* s, u32 a, float g, const float (&b)[8], rsrc_t rO, u32 vo, u32 so) {
  f32x4 lo = *(f32x4*)(s + a), hi = *(f32x4*)(s + a + 256);
#pragma unroll
  for (int q = 1; q < 4; ++q) { lo += *(f32x4*)(s + a + q * 10880); hi += *(f32x4*)(s + a + q * 10880 + 256); }
  u32x4 o = {pk(g * lo[0] + b[0], g * lo[1] + b[1]), pk(g * lo[2] + b[2], g * lo[3] + b[3]),
             pk(g * hi[0] + b[4], g * hi[1] + b[5]), pk(g * hi[2] + b[6], g * hi[3] + b[7])};
  __builtin_amdgcn_raw_buffer_store_b128(o, rO, vo, so, 0);
}

__global__ __launch_bounds__(512, 2) void fused_kernel(u64 px, u64 pw, u64 pws, u64 pwo, u64 pwr, u64 pgw, u64 pwg, u64 pwu, u64 pwd, u64 pout) {
  extern __shared__ __attribute__((aligned(16))) char smem[];
  char* s = smem;
  const int tid = threadIdx.x, lane = tid & 63;
  u32 pf;
  {
    u64 pc; u32 len;
    asm volatile("s_getpc_b64 %0\nfk_code_lo:\n\ts_mov_b32 %1, fk_code_hi-fk_code_lo" : "=s"(pc), "=s"(len));
    asm volatile("buffer_load_dword %0, %1, %2, 0 offen" : "=&v"(pf) : "v"(tid >= 448 ? (tid - 448 + (blockIdx.x >> 3 & 3) * 64) * 128 : OOB), "s"(mk_rsrc(pc, len)) : "memory");
  }
  const int w = (int)rfl(tid >> 6);
  asm volatile("" : "+s"(px), "+s"(pw), "+s"(pws), "+s"(pwo), "+s"(pwr), "+s"(pgw), "+s"(pwg), "+s"(pwu), "+s"(pwd), "+s"(pout));
  const int b = blockIdx.x, xcd = b & 7, loc = b >> 3;
  const u32 sb = rfl((u32)(size_t)(LDSP(char))smem), sb1 = sb + w * 1024;
  rsrc_t ws = mk_rsrc(pws, WS_END);
  const int kq = lane >> 4, l15 = lane & 15, qp = (lane >> 2) & 3, pp = lane & 3;
  u32 epv; ld32_sc1(epv, ws, WS_EP, 0);
  if (tid < 16) *(u32*)(s + L_CNT + tid * 4) = 0;
  const int r = (xcd >> 1) * 4 + (loc >> 3), ct = (xcd & 1) * 8 + (loc & 7);
  const int h = xcd * 4 + (loc & 3), gp = loc >> 2;
  const int sl = loc & 7, oi = loc >> 3;
  const int rot = ((loc >> 3) * 10 / 4 + (loc & 7) * 10 / 8 + xcd) % 10;
  u32 tag, flv; u32x2 selv;
  rsrc_t rX = mk_rsrc(px, 2048 * 1280), rWo = mk_rsrc(pwo, 640 * 1280);
  u32 vwo[5];
#pragma unroll
  for (int i = 0; i < 5; ++i) { const u32 u = i * 64 + lane, q = (u * 6554) >> 16; vwo[i] = ((q & 19) | (q >> 1 & 4) | (q << 1 & 8)) * 1280 + (u - q * 10) * 16; }
  const u32 swo = w * 40960 + sl * 160, sb5 = sb + w * 5120;
  auto wo_piece = [&](auto gc, auto ic) __attribute__((always_inline)) { constexpr int g = decltype(gc)::v, i = decltype(ic)::v;
    dma<g * 5120 + i * 1024>(rWo, vwo[i], swo + g * 40960, sb5); };
  const int c0 = (5 * h) >> 1, krow = 2 * gp - 4 + w;
  const u32 fcol = ((c0 + 2 * (lane & 1)) >> 4) * 128;
  const u32 flq = WS_FG + (2 * gp + (w >> 2)) * 2048 + fcol, flk = WS_FG + krow * 2048 + (lane & 2 ? 1280 : 640) + fcol;
  const u32 fl1 = (lane < 4 && w < 6) ? flk : flq, fl0 = krow < 0 ? flq : fl1, selo = WS_SEL + r * 128 + (lane & 7) * 8;
  const u64 pfw = w < 2 ? pwg + 0 : pwu + 0;
  auto pre = [&](auto ic) __attribute__((always_inline)) { wo_piece(IC<0>{}, ic); };
  u32x2 gr; u32 ckey;
  if (ct < 15) g_phase(rX, pw, s, sb, ws, w, lane, r, ct, rot, tag, epv, pf, flv, fl0, pre STAMP_PASS);
  else r_phase(rX, pwr, pgw, s, sb, ws, w, lane, tid, r, tag, epv, pf, flv, fl0, gr, ckey, pre STAMP_PASS);

  STAMP(3, 22);
  PIN(flv); spin_flags(flv, ws, fl0, 0, tag);
  STAMP(22, 4);
  const u32 skv = WS_QKV + (80 + c0) * 32768 + krow * 1024;
  const u32 vq = (c0 + (kq > 2 ? 2 : kq)) * 32768 + (gp * 128 + w * 16 + l15) * 16;
  u32x4 q0, q1, xg[4];
  if (w < 6) attn_stage<L_A0>(ws, sb1, krow < 0 ? OOB : skv, lane);
  ld128_sc1(q0, ws, vq, WS_QKV);
  STAMP(4, 23);
  const bool f1 = loc < 30 && w < 4;
  if (ct == 15) r_select(s, ws, w, tid, r, tag, gr, ckey);
  else if (w < 6) WAITV(7); else WAITV(1);
  arrive_flag(s, 1, ws, ct < 15 ? WS_FG + ((16 + r) * 16 + ct) * 128 : OOB, tag, lane);
  STAMP(23, 5);
  const int odd = h & 1;
  const u32x2 ones = {0x3f803f80u, 0x3f803f80u};
  const u32 aone = 18432 + (odd ? 0 : 12544 + 8) + w * 1024 + lane * 16;
  if (ct == 15) WAITV(3); else WAITV(1);
  STAMP(5, 6);
  if (w < 6 && krow >= 0) *(u32x2*)(s + L_A0 + aone) = ones;
  const bool qlo = kq == 3 || (odd && kq == 0), qhi = kq == 3 || (!odd && kq == 2);
  PIN(q0); q0[0] = qlo ? 0u : q0[0]; q0[1] = qlo ? 0u : q0[1]; q0[2] = qhi ? 0u : q0[2]; q0[3] = qhi ? 0u : q0[3];
  const u32 vrow = L_A0 + 18432 + (16 * w + 4 * kq + qp) * 16;
  u32 aK = L_A0 + (kq > 2 ? 2 : kq) * 6144 + w * 256 + l15 * 16;
  u32 aV0 = vrow + ((pp + odd) >> 1) * 6272 + ((pp + odd) & 1) * 8;
  u32 aV1 = vrow + (pp == 0 ? 12544 + odd * 8 : (odd ? 0 : 12544 + 8));
  PIN(aK); PIN(aV0); PIN(aV1);
  u32 vo0 = (c0 + ((kq + odd) >> 1)) * 32768 + ((kq + odd) & 1) * 8 + l15 * 16, vo1 = kq == 0 ? (c0 + 2) * 32768 + odd * 8 + l15 * 16 : OOB;
  const u32 so = WS_O + (gp * 128 + w * 16) * 16;
  PIN(vo0); PIN(vo1);
  const u32 a32 = ((kq >> 1) * 16 + (kq & 1) * 4 + qp) * 32 + pp * 8;
  const int s64 = xcd * 3 + loc / 10, cgp = loc % 10;
  u32x4 fw[4];
  BAR();
  STAMP(6, 7);
  attn_unit(s, q0, aK, aV0, aV1, gp < 2 ? (16 - gp * 8 - w > 0 ? 16 - gp * 8 - w : 0) : 0, lane, ws, vo0, vo1, so, [&](auto bc) { constexpr int b = decltype(bc)::v;
    if constexpr (b < 3) {
      if (w < 4) {
        if (b < 2 && f1) {
          rsrc_t rr = mk_rsrc(pfw, 640 * 3072);
          const u32 vo = ((lane >> 1 & 19) | (lane >> 2 & 4) | (lane & 8)) * 3072 + (lane & 1) * 16;
          const u32 so = ((loc % 5) * 128 + b * 64) * 3072 + (xcd * 6 + loc / 5) * 64 + (w & 1) * 32;
          dma<L_F1W + b * 2048>(rr, vo, so, sb5); dma<L_F1W + b * 2048 + 1024>(rr, vo, so + 32 * 3072, sb5);
        }
      } else if (w < 7) { wo_piece(IC<8>{}, IC<2 * b>{}); if constexpr (b < 2) wo_piece(IC<8>{}, IC<2 * b + 1>{}); }
    }
    if constexpr (b == 7) if (w < 4) { __builtin_amdgcn_s_waitcnt(0xC07F); PIN(fw[0]); PIN(fw[1]); PIN(fw[2]); PIN(fw[3]); wo_piece(IC<8>{}, IC<0>{}); wo_piece(IC<8>{}, IC<1>{}); }
    if constexpr (b == 7) dma<L_F2W>(mk_rsrc(pwd, 1536 * 1280), (lane >> 3) * 1280 + (((lane & 7) ^ (2 * ((kq & 1) | ((w & 1) << 1)))) * 16), (s64 * 64 + 8 * w) * 1280 + cgp * 128, sb1);
    if constexpr (b == 3) { STAMP(7, 24); ld32_sc1(flv, ws, fl1, 16 * 2048); }
    if constexpr (b == 5) {
      STAMP(24, 25);
      WAITV(0); PIN(flv); spin_flags(flv, ws, fl1, 16 * 2048, tag);
      STAMP(25, 26);
      if (w < 6) attn_stage<L_A1>(ws, sb1, skv + 16 * 1024, lane);
      ld128_sc1(q1, ws, vq, WS_QKV + 1024 * 16);
      ld64_sc1(selv, ws, selo, 0);
    }
    if constexpr (b == 6) {
#pragma unroll
      for (int ks = 0; ks < 4; ++ks) fw[ks] = cat(trrd(s, L_F1W + w * 5120 + a32 + ks * 1024), trrd(s, L_F1W + w * 5120 + a32 + ks * 1024 + 256));
    }
  });
  STAMP(26, 8);
  if (w < 4) WAITV(5); else WAITV(3);
  PIN(selv); spin_gran(selv, ws, selo, 0, tag);
  const u32 selkey = selv[0], seltok = 2047 - (selkey & 0xffffu);
  if (f1) {
    const u32 vo = l15 < 8 ? seltok * 1280 + kq * 16 : OOB, so = (loc % 5) * 256;
    ld128<0>(xg[0], rX, vo, so); ld128<64>(xg[1], rX, vo, so); ld128<128>(xg[2], rX, vo, so); ld128<192>(xg[3], rX, vo, so);
  }
  aK += L_A1 - L_A0; aV0 += L_A1 - L_A0; aV1 += L_A1 - L_A0; PIN(aK); PIN(aV0); PIN(aV1);
  if (w < 6) *(u32x2*)(s + L_A1 + aone) = ones;
  PIN(q1); q1[0] = qlo ? 0u : q1[0]; q1[1] = qlo ? 0u : q1[1]; q1[2] = qhi ? 0u : q1[2]; q1[3] = qhi ? 0u : q1[3];
  BAR();
  STAMP(8, 9);
  const bool f2 = loc < 30;
  u32x4 pp1[10];
  const u32 vp1 = WS_P1 + (2 * s64 + (w & 1)) * 10240 + lane * 16;
  auto p1load = [&]() { static_for<10>([&](auto ic) { constexpr int i = decltype(ic)::v; ld128_sc1<(i & 3) * 1024>(pp1[i], ws, vp1, (i >> 2) * 4096); }); };
  const int tokA = 128 * xcd + 32 * oi;
  const u32 fa = WS_FA + (xcd * 32 + (lane & 31)) * 128;
  const int tk = (tid * 6554) >> 16, pc = tid - tk * 10;
  u32x4 of[5], og[5], xv0, xv1; u32x2 g0, g1; u32 ef[4];
  const u32 xo = tid < 320 ? (tokA + tk) * 1280 + sl * 160 + pc * 16 : OOB, go = WS_GG + (tokA + (tk & 31)) * 8;
  const u32 eo = (lane & 15) < 15 ? WS_FG + lane * 128 : WS_CAND + kq * 128 + 4, es = (lane & 15) < 15 ? 8192 : 512;
  const bool ew = b == 0 && w == 5;
  attn_unit(s, q1, aK, aV0, aV1, 0, lane, ws, vo0, vo1, so + 1024 * 16, [&](auto bc) { constexpr int b = decltype(bc)::v;
    if constexpr (b == 0) if (w < 4) { wo_piece(IC<8>{}, IC<2>{}); wo_piece(IC<8>{}, IC<3>{}); wo_piece(IC<8>{}, IC<4>{}); }
    if constexpr (b < 2) if (w >= 3) { wo_piece(IC<12>{}, IC<2 * b>{}); wo_piece(IC<12>{}, IC<2 * b + 1>{}); if (b == 1) wo_piece(IC<12>{}, IC<4>{}); }
    if constexpr (b == 2) {
      if (f1) {
        if (w < 3) WAITV(3); else WAITV(8);
        f32x4 c = {0.f, 0.f, 0.f, 0.f};
#pragma unroll
        for (int ks = 0; ks < 4; ++ks) { PIN(xg[ks]); c = mfma(fw[ks], xg[ks], c); }
        const u32 po = WS_P1 + (((xcd * 6 + loc / 5) * 5 + loc % 5) * 2 + (w >> 1)) * 1024 + (w & 1) * 512 + kq * 128 + l15 * 16;
        st128_sc1(gran_pack(c, tag), ws, l15 < 8 ? po : OOB);
      }
    }
    if constexpr (b == 3) {
      if (w >= 4) WAITV(5); else if (!f1) WAITV(3); else if (w < 3) WAITV(8); else WAITV(13);
      arrive_flag(s, 2, ws, WS_FA + (gp * 32 + h) * 128, tag, lane);
      ld128(xv0, rX, xo, 0); ld128(xv1, rX, xo, 1024 * 1280); ld64_sc1(g0, ws, go, 0); ld64_sc1(g1, ws, go, 8192);
    }
    if constexpr (b == 6) {
      if (ew) {
#pragma unroll
        for (int i = 0; i < 4; ++i) ld32_sc1(ef[i], ws, eo + i * es, 0);
      }
    }
    if constexpr (b == 7) { ld32_sc1(flv, ws, fa, 0); if (w >= 6) p1load(); }
  });
  STAMP(9, 10);
  WAITV(2);
  const u32 ofo = (w >> 1) * 20 * 32768 + kq * 32768 + (tokA + (w & 1) * 16 + l15) * 16;
  auto ldo0 = [&]() {
#pragma unroll
    for (int ks = 0; ks < 5; ++ks) ld128_sc1(of[ks], ws, ofo, WS_O + ks * 4 * 32768); };
  PIN(flv);
  const bool rdy0 = __all(flv == tag);
  if (rdy0) ldo0();
  STAMP(10, 27);
  if (f2 && w >= 6) {
#pragma unroll 1
    for (int it = 0; it < SPIN_MAX; ++it) {
      bool ok = true;
#pragma unroll
      for (int i = 0; i < 10; ++i) { PIN(pp1[i]); ok = ok && pp1[i][3] == tag; }
      if (__all(ok)) break;
      p1load(); WAITV(0);
    }
    f32x4 g = gran_unpack(pp1[0]), u = gran_unpack(pp1[1]);
#pragma unroll
    for (int i = 1; i < 5; ++i) { g += gran_unpack(pp1[2 * i]); u += gran_unpack(pp1[2 * i + 1]); }
    u32x2 hv = {pk(g[0] * sigmoidf_(g[0]) * u[0], g[1] * sigmoidf_(g[1]) * u[1]), pk(g[2] * sigmoidf_(g[2]) * u[2], g[3] * sigmoidf_(g[3]) * u[3])};
    *(u32x2*)(s + L_H + (lane & 7) * 128 + (w & 1) * 64 + (lane >> 3) * 8) = hv;
  }
  if (ew) {
#pragma unroll
    for (int i = 0; i < 4; ++i) { PIN(ef[i]); spin_flags(ef[i], ws, eo + i * es, 0, tag); }
    if (lane == 0) st32_sc1(tag, ws, WS_EP);
  }
  BAR();
  STAMP(27, 28);
  const u32 fsw = ((qp >> 1) | ((kq & 1) << 1)) * 32 + pp * 8;
  if (f1) {
    const u32 a = L_F2W + (8 * kq + qp) * 128 + (fsw ^ (w * 32)), ah = L_H + (l15 & 7) * 128 + kq * 16;
    f32x4 c = {0.f, 0.f, 0.f, 0.f};
    c = mfma(cat(trrd(s, a), trrd(s, a + 512)), lds128(s, ah), c);
    c = mfma(cat(trrd(s, a + 4096), trrd(s, a + 4096 + 512)), lds128(s, ah + 64), c);
    st128_sc1(gran_pack(c, tag), ws, l15 < 8 ? WS_Y + ((s64 * 8 + l15) * 160 + cgp * 16 + w * 4 + kq) * 16 : OOB);
  }
  u32x4 bw[5][5];
  { u32 a = (w >> 1) * 25600 + ((kq >> 1) * 16 + (kq & 1) * 4 + qp) * 160 + pp * 8;
    PIN(a);
#pragma unroll
    for (int ks = 0; ks < 5; ++ks) {
#pragma unroll
      for (int nt = 0; nt < 5; ++nt) bw[ks][nt] = cat(trrd(s, a + ks * 5120 + nt * 32), trrd(s, a + ks * 5120 + nt * 32 + 1280));
      if (ks == 1) {
        SCHED();
        if (rdy0) { if (f1) WAITV(6); else WAITV(5); } else if (f1) WAITV(1); else WAITV(0);
        arrive_flag(s, 3, ws, WS_FA + ((8 + gp) * 32 + h) * 128, tag, lane);
        if (!rdy0) { u32 t = ~tag; spin_flags(t, ws, fa, 0, tag); ldo0(); }
        STAMP(28, 11);
        SCHED();
      }
    } }
  PIN(g0); PIN(g1); PIN(xv0); PIN(xv1);
  spin_gran(g0, ws, go, 0, tag); spin_gran(g1, ws, go, 8192, tag);
  const float ga = __uint_as_float(g0[0]), gb = __uint_as_float(g1[0]);
  float ba[8], bb[8];
#pragma unroll
  for (int e = 0; e < 4; ++e) {
    ba[2 * e] = (1.f - ga) * blo(xv0[e]); ba[2 * e + 1] = (1.f - ga) * bhi(xv0[e]);
    bb[2 * e] = (1.f - gb) * blo(xv1[e]); bb[2 * e + 1] = (1.f - gb) * bhi(xv1[e]);
  }
  u32 ared = w * 5440 + (lane >> 5) * 544 + (lane & 31) * 16, afin = (tk >> 4) * 5440 + (pc >> 1) * 1088 + (pc & 1) * 544 + (tk & 15) * 16;
  u32 ared1 = ared + 43520, afin1 = afin + 43520;
  PIN(ared); PIN(afin); PIN(ared1); PIN(afin1);
  rsrc_t rO = mk_rsrc(pout, 2048 * 1280);
  ld32_sc1(flv, ws, fa, 256 * 128);
  const u32 m0 = (u32)__ballot(lane < 8 && ((seltok & 1023u) - (u32)tokA) < 32u);
  const int sp = (tid * 3277) >> 16;
  const u32 yb0 = WS_Y + ((sp < 24 ? sp : 0) * 1280 + sl * 20 + tid - sp * 20) * 16;
  u32x4 y;
  if (m0) ld128_sc1(y, ws, yb0, __builtin_ctz(m0) * 2560);
  BAR();
  STAMP(11, 12);
  if (m0) WAITV(2); else WAITV(1);
#pragma unroll
  for (int ks = 0; ks < 5; ++ks) PIN(of[ks]);
  o_compute(s, ared, bw, of);
  WAITV(0); PIN(flv);
  const bool rdy = __all(flv == tag);
  auto ldo1 = [&]() {
#pragma unroll
    for (int ks = 0; ks < 5; ++ks) ld128_sc1(og[ks], ws, ofo, WS_O + ks * 4 * 32768 + 1024 * 16); };
  for (u32 m = m0; m; m &= m - 1) {
    const int c = __builtin_ctz(m);
    if (m != m0) { ld128_sc1(y, ws, yb0, c * 2560); WAITV(0); }
    PIN(y);
#pragma unroll 1
    for (int it = 0; it < SPIN_MAX && !__all(y[3] == tag); ++it) { ld128_sc1(y, ws, yb0, c * 2560); WAITV(0); PIN(y); }
    if (tid < 480) *(f32x4*)(s + L_Y + c * 7680 + tid * 16) = gran_unpack(y);
  }
  if (rdy) ldo1(); else ld32_sc1(flv, ws, fa, 256 * 128);
  STAMP(12, 13);
  BAR();
  for (u32 m = m0; m; m &= m - 1) {
    const int c = __builtin_ctz(m);
    const u32 key = __builtin_amdgcn_readlane(selkey, c), tok = 2047 - (key & 0xffffu);
    if (tid < 320 && (u32)(tokA + tk) == (tok & 1023u)) {
      const u32 o = key >> 16, rb = (o & 0x8000u) ? (o ^ 0x8000u) : (o ^ 0xffffu), a = L_Y + c * 7680 + pc * 32;
      const float rs = sigmoidf_(__uint_as_float(rb << 16));
      f32x4 ya = *(f32x4*)(s + a), yb = *(f32x4*)(s + a + 16);
#pragma unroll
      for (int q = 1; q < 24; ++q) { ya += *(f32x4*)(s + a + q * 320); yb += *(f32x4*)(s + a + q * 320 + 16); }
      const float fa_ = tok < 1024 ? (1.f - ga) * rs : 0.f, fb_ = tok < 1024 ? 0.f : (1.f - gb) * rs;
#pragma unroll
      for (int e = 0; e < 4; ++e) { ba[e] += fa_ * ya[e]; ba[4 + e] += fa_ * yb[e]; bb[e] += fb_ * ya[e]; bb[4 + e] += fb_ * yb[e]; }
    }
  }
  o_final(s, afin, ga, ba, rO, xo, 0);
  STAMP(13, 14);
  if (rdy) WAITV(1); else { WAITV(1); PIN(flv); spin_flags(flv, ws, fa, 256 * 128, tag); ldo1(); WAITV(0); }
#pragma unroll
  for (int ks = 0; ks < 5; ++ks) PIN(og[ks]);
  STAMP(14, 15);
  o_compute(s, ared1, bw, og);
  BAR();
  STAMP(15, 16);
  if (w < 5) o_final(s, afin1, gb, bb, rO, xo, 1024 * 1280);
  STAMP(16, 17);
  asm volatile("fk_code_hi:" ::: "memory");
}

extern "C" void kernel_launch(void* const* d_in, const int* in_sizes, int n_in, void* d_out, int out_size, void* d_ws, size_t ws_size,
                              hipStream_t stream) {
  (void)hipFuncSetAttribute((const void*)fused_kernel, hipFuncAttributeMaxDynamicSharedMemorySize, LDS_BYTES);
  fused_kernel<<<256, 512, LDS_BYTES, stream>>>((u64)d_in[0], (u64)d_in[1], (u64)d_ws, (u64)d_in[2], (u64)d_in[4], (u64)d_in[8],
                                                (u64)d_in[5], (u64)d_in[6], (u64)d_in[7], (u64)d_out);
}
```

```cpp
#include <hip/hip_runtime.h>
#include <hip/hip_bf16.h>

typedef unsigned short u16;
typedef unsigned int u32;
typedef unsigned long long u64;
typedef __attribute__((ext_vector_type(2))) u32 u32x2;
typedef __attribute__((ext_vector_type(4))) u32 u32x4;
typedef __attribute__((ext_vector_type(4))) float f32x4;
typedef __attribute__((ext_vector_type(8))) __bf16 bf16x8;
typedef __attribute__((ext_vector_type(2))) __bf16 bf16x2;
typedef __attribute__((ext_vector_type(4))) short s16x4;
#define LDSP(T) __attribute__((address_space(3))) T*
typedef __amdgpu_buffer_rsrc_t rsrc_t;
#define DEV __device__ __forceinline__

constexpr u32 WS_QKV = 0;
constexpr u32 WS_O   = WS_QKV + 240u * 32768;
constexpr u32 WS_P1  = WS_O + 80u * 32768;
constexpr u32 WS_Y   = WS_P1 + 480u * 1024;
constexpr u32 WS_FG  = WS_Y + 24u * 8 * 160 * 16;
constexpr u32 WS_FA  = WS_FG + 512u * 128;
constexpr u32 WS_CAND = WS_FA + 512u * 128;
constexpr u32 WS_SEL = WS_CAND + 16u * 128;
constexpr u32 WS_GG  = WS_SEL + 16u * 128;
constexpr u32 WS_EP  = WS_GG + 2048u * 8;
constexpr u32 WS_END = WS_EP + 128;
constexpr u32 WS_STAMP = WS_END;

constexpr u32 L_X1   = 73728;
constexpr u32 L_F1W  = 40960;
constexpr u32 L_A0   = 76800;
constexpr u32 L_A1   = 114048;
constexpr u32 L_F2W  = 151296;
constexpr u32 L_RW   = 155648;
constexpr u32 L_CNT  = 159488;
constexpr u32 L_H    = 159744;
constexpr u32 L_KEYS = 160768;
constexpr u32 L_RB   = 161280;
constexpr u32 L_Y    = 87040;
constexpr u32 LDS_BYTES = 163840;
constexpr int SPIN_MAX = 1 << 14;
constexpr u32 OOB = 0x80000000u;

#define STAMP(prev, i) do {} while (0)
#define STAMP_ARG
#define STAMP_PASS

DEV rsrc_t mk_rsrc(u64 p, u32 bytes) { return __builtin_amdgcn_make_buffer_rsrc((void*)p, 0, bytes, 0x00020000); }
DEV u32 rfl(u32 v) { return (u32)__builtin_amdgcn_readfirstlane((int)v); }
template <u32 L> DEV void dma(rsrc_t r, u32 voff, u32 soff, u32 lds) {
  asm volatile("s_add_u32 m0, %0, %4\n\ts_nop 0\n\tbuffer_load_dwordx4 %1, %2, %3 offen lds" :: "s"(lds), "v"(voff), "s"(r), "s"(soff), "n"(L) : "memory", "scc");
}
template <u32 L> DEV void dma_sc1(rsrc_t r, u32 voff, u32 soff, u32 lds) {
  asm volatile("s_add_u32 m0, %0, %4\n\ts_nop 0\n\tbuffer_load_dwordx4 %1, %2, %3 offen sc1 lds" :: "s"(lds), "v"(voff), "s"(r), "s"(soff), "n"(L) : "memory", "scc");
}
template <u32 L> DEV void dma3(rsrc_t rX, rsrc_t rW, u32 vX, u32 vW0, u32 vW1, u32 sX, u32 sW, u32 lds) {
  asm volatile("s_add_u32 m0, %0, %8\n\ts_nop 0\n\tbuffer_load_dwordx4 %1, %2, %3 offen lds\n\t"
               "buffer_load_dwordx4 %4, %6, %7 offen offset:1024 lds\n\tbuffer_load_dwordx4 %5, %6, %7 offen offset:2048 lds"
               :: "s"(lds), "v"(vX), "s"(rX), "s"(sX), "v"(vW0), "v"(vW1), "s"(rW), "s"(sW), "n"(L) : "memory", "scc");
}
template <u32 I> DEV void dma_cont(rsrc_t r, u32 voff, u32 soff) {
  asm volatile("buffer_load_dwordx4 %0, %1, %2 offen offset:%3 lds" :: "v"(voff), "s"(r), "s"(soff), "n"(I) : "memory");
}
template <u32 I = 0> DEV void ld128_sc1(u32x4& d, rsrc_t r, u32 voff, u32 soff) { asm volatile("buffer_load_dwordx4 %0, %1, %2, %3 offen offset:%4 sc1" : "=&v"(d) : "v"(voff), "s"(r), "s"(soff), "n"(I) : "memory"); }
template <u32 I = 0> DEV void ld128(u32x4& d, rsrc_t r, u32 voff, u32 soff) { asm volatile("buffer_load_dwordx4 %0, %1, %2, %3 offen offset:%4" : "=&v"(d) : "v"(voff), "s"(r), "s"(soff), "n"(I) : "memory"); }
template <u32 I> DEV void gld128(u32x4& d, const char* a) { asm volatile("global_load_dwordx4 %0, %1, off offset:%2" : "=&v"(d) : "v"(a), "n"(I) : "memory"); }
DEV void ld64_sc1(u32x2& d, rsrc_t r, u32 voff, u32 soff) { asm volatile("buffer_load_dwordx2 %0, %1, %2, %3 offen sc1" : "=&v"(d) : "v"(voff), "s"(r), "s"(soff) : "memory"); }
DEV void ld32_sc1(u32& d, rsrc_t r, u32 voff, u32 soff) { asm volatile("buffer_load_dword %0, %1, %2, %3 offen sc1" : "=&v"(d) : "v"(voff), "s"(r), "s"(soff) : "memory"); }
#define WAITV(n) asm volatile("s_waitcnt vmcnt(%0)" :: "n"(n) : "memory")
template <class T> DEV void pin_(T& x) { asm volatile("" : "+v"(x)); }
#define PIN(x) pin_(x)
#define BAR() do { asm volatile("" ::: "memory"); __builtin_amdgcn_s_waitcnt(0xC07F); asm volatile("s_barrier" ::: "memory"); } while (0)
#define SCHED() __builtin_amdgcn_sched_barrier(0)
DEV void st64_sc1(u32x2 v, rsrc_t r, u32 voff, u32 soff = 0) { __builtin_amdgcn_raw_buffer_store_b64(v, r, voff, soff, 16); }
DEV void st128_sc1(u32x4 v, rsrc_t r, u32 voff) { __builtin_amdgcn_raw_buffer_store_b128(v, r, voff, 0, 16); }
DEV void st32_sc1(u32 v, rsrc_t r, u32 voff) { __builtin_amdgcn_raw_buffer_store_b32(v, r, voff, 0, 16); }

DEV u32 pk(float a, float b) { bf16x2 v = {(__bf16)a, (__bf16)b}; return __builtin_bit_cast(u32, v); }
DEV float rbf(float a) { return (float)(__bf16)a; }
DEV float blo(u32 v) { return __uint_as_float(v << 16); }
DEV float bhi(u32 v) { return __uint_as_float(v & 0xffff0000u); }
DEV f32x4 mfma(u32x4 a, u32x4 b, f32x4 c) { return __builtin_amdgcn_mfma_f32_16x16x32_bf16(__builtin_bit_cast(bf16x8, a), __builtin_bit_cast(bf16x8, b), c, 0, 0, 0); }
DEV u32x2 trrd(char* s, u32 a) { return __builtin_bit_cast(u32x2, __builtin_amdgcn_ds_read_tr16_b64_v4i16((LDSP(s16x4))(s + a))); }
DEV u32x4 lds128(char* s, u32 a) { return *(u32x4*)(s + a); }
DEV u32x4 cat(u32x2 a, u32x2 b) { u32x4 r = {a[0], a[1], b[0], b[1]}; return r; }
DEV float ex2(float v) { return __builtin_amdgcn_exp2f(v); }
DEV float sigmoidf_(float v) { return __builtin_amdgcn_rcpf(1.f + ex2(v * -1.4426950408889634f)); }
DEV u32x4 gran_pack(f32x4 c, u32 tag) {
  const u32 a = __float_as_uint(c[0]), b = __float_as_uint(c[1]), d = __float_as_uint(c[2]), e = __float_as_uint(c[3]);
  u32x4 r = {__builtin_amdgcn_perm(a, e, 0x07060503u), __builtin_amdgcn_perm(b, e, 0x07060502u), __builtin_amdgcn_perm(d, e, 0x07060501u), tag};
  return r;
}
DEV f32x4 gran_unpack(u32x4 g) {
  const u32 t = __builtin_amdgcn_perm(g[0], g[1], 0x04000c0cu);
  f32x4 r = {__uint_as_float(g[0]), __uint_as_float(g[1]), __uint_as_float(g[2]), __uint_as_float(__builtin_amdgcn_perm(t, g[2], 0x0706000cu))};
  return r;
}

DEV void arrive_flag(char* s, int cnt, rsrc_t ws, u32 flagoff, u32 tag, int lane) {
  if (lane == 0) {
    u32 old;
    asm volatile("ds_add_rtn_u32 %0, %1, %2\n\ts_waitcnt lgkmcnt(0)" : "=v"(old) : "v"((u32)(size_t)(LDSP(char))(s + L_CNT + cnt * 4)), "v"(1u) : "memory");
    st32_sc1(tag, ws, old == 7 ? flagoff : OOB);
  }
}
DEV void spin_flags(u32 v, rsrc_t ws, u32 voff, u32 soff, u32 tag) {
#pragma unroll 1
  for (int i = 0; i < SPIN_MAX && !__all(v == tag); ++i) { ld32_sc1(v, ws, voff, soff); WAITV(0); PIN(v); }
}
DEV void spin_gran(u32x2& v, rsrc_t ws, u32 voff, u32 soff, u32 tag) {
#pragma unroll 1
  for (int i = 0; i < SPIN_MAX && !__all(v[1] == tag); ++i) { ld64_sc1(v, ws, voff, soff); WAITV(0); PIN(v); }
}
DEV u32 rank128(char* s, int tid, u32& key) {
  key = *(u32*)(s + L_KEYS + (tid >> 2) * 4);
  u32 c = 0;
#pragma unroll
  for (int i = 0; i < 8; ++i) {
    u32x4 k = lds128(s, L_KEYS + (tid & 3) * 128 + i * 16);
    c += (k[0] > key) + (k[1] > key) + (k[2] > key) + (k[3] > key);
  }
  c += __builtin_amdgcn_update_dpp(0u, c, 0xB1, 0xf, 0xf, true);
  c += __builtin_amdgcn_update_dpp(0u, c, 0x4E, 0xf, 0xf, true);
  return c;
}

DEV void g_epilogue(f32x4 (&acc)[2][2], rsrc_t ws, u32 voff, u32 soff, bool isq) {
  if (isq) {
#pragma unroll
    for (int i = 0; i < 4; ++i) acc[i >> 1][i & 1] *= 0.32259863752095353f;
  }
#pragma unroll
  for (int ft = 0; ft < 2; ++ft)
#pragma unroll
    for (int tt = 0; tt < 2; ++tt) {
      f32x4 a = acc[ft][tt];
      u32x2 v = {pk(a[0], a[1]), pk(a[2], a[3])};
      st64_sc1(v, ws, voff + tt * 256, soff + ft * 65536);
    }
}
constexpr u32 ring_slot(int i) { return (i & 3) == 3 ? L_X1 + 7 * 8192 : (i & 3) * 24576; }
template <int I> struct IC { static constexpr int v = I; };
template <int N, class F> DEV void static_for(F&& f) { if constexpr (N > 0) { static_for<N - 1>(f); f(IC<N - 1>{}); } }

template <class Pre>
DEV void g_phase(rsrc_t rX, u64 pw, char* s, u32 sb, rsrc_t ws, int w, int lane, int r, int ct, int rot,
                 u32& tag, u32 epv, u32 pf, u32& flv, u32 fl0, Pre&& pre STAMP_ARG) {
  const int cg = w & 3, th = w >> 2, kq = lane >> 4, l15 = lane & 15;
  rsrc_t rW = mk_rsrc(pw - 2048, 640 * 3840 + 2048);
  const u32 vW = kq * 3840 + ((l15 ^ (2 * (kq | ((w & 1) << 2)))) * 16), vW0 = vW + 1024, vW1 = vW + 4 * 3840;
  const u32 vX = (lane >> 3) * 1280 + (((lane & 7) ^ ((4 * w + kq) & 7)) * 16);
  const u32 sW = 8 * w * 3840 + ct * 256, sX = (64 * r + 8 * w) * 1280, sb3 = sb + w * 3072, sb1 = sb + w * 1024;
  u32 k0 = rot * 128, k1 = rot * 128;
  auto batch = [&](auto ic) { constexpr int i = decltype(ic)::v;
    dma3<ring_slot(i)>(rX, rW, vX, vW0, vW1, sX + k0, sW + k0 * 1920, sb3); k0 = k0 == 1152 ? 0 : k0 + 128; };
  auto x1 = [&](auto jc) { constexpr int j = decltype(jc)::v;
    dma<L_X1 + j * 8192>(rX, vX, sX + k1 + 1024 * 1280, sb1); k1 = k1 == 1152 ? 0 : k1 + 128; };
  STAMP(0, 18);
  batch(IC<0>{});
  asm volatile("s_barrier" ::: "memory");
  batch(IC<1>{});
  asm volatile("s_barrier" ::: "memory");
  batch(IC<2>{});
  STAMP(18, 19);
  const int qp = (lane >> 2) & 3, pp = lane & 3, hrow = qp | ((kq & 1) << 2);
  u32 aW0 = kq * 3072 + 1024 + qp * 256 + (((cg * 4 + (pp >> 1)) ^ (2 * hrow)) * 16) + (pp & 1) * 8, aW1 = aW0 ^ 32;
  const u32 xsw = (kq ^ (l15 >> 1)) * 16;
  u32 aR0 = (4 * th + (l15 >> 3)) * 3072 + (l15 & 7) * 128 + xsw, aR1 = aR0 ^ 64;
  u32 aS0 = L_X1 + (32 * th + l15) * 128 + xsw, aS1 = aS0 ^ 64;
  PIN(aW0); PIN(aW1); PIN(aR0); PIN(aR1); PIN(aS0); PIN(aS1);
  u32x4 wf[20][2];
  f32x4 acc[2][2] = {}, acd[2][2] = {};
  u32x4 xf[2][4], xh[10][4];
  auto mm0 = [&](auto ic, int h) { constexpr int i = decltype(ic)::v;
#pragma unroll
    for (int q = 0; q < 4; ++q) acc[q & 1][q >> 1] = mfma(wf[2 * i + h][q & 1], xf[i & 1][2 * h + (q >> 1)], acc[q & 1][q >> 1]); };
  auto rd1 = [&](int j) {
#pragma unroll
    for (int q = 0; q < 4; ++q) xh[j][q] = lds128(s, ((q >> 1) ? aS1 : aS0) + (j < 6 ? j : j - 6) * 8192 + (q & 1) * 2048); };
  auto mm1 = [&](int j) {
#pragma unroll
    for (int q = 0; q < 8; ++q) acd[q & 1][q >> 1 & 1] = mfma(wf[2 * j + (q >> 2)][q & 1], xh[j][q >> 1], acd[q & 1][q >> 1 & 1]); };
  static_for<10>([&](auto ic) { constexpr int i = decltype(ic)::v;
    constexpr u32 base = (i & 3) == 3 ? 0 : ring_slot(i), jump = (i & 3) == 3 ? ring_slot(3) : (i & 3) == 0 && i ? -ring_slot(3) : 0;
    if constexpr (jump != 0) { aW0 += jump; aW1 += jump; aR0 += jump; aR1 += jump; PIN(aW0); PIN(aW1); PIN(aR0); PIN(aR1); }
    const u32 kx = sX + k0, kw = sW + k0 * 1920;
    k0 = k0 == 1152 ? 0 : k0 + 128;
    WAITV(i < 8 ? 6 : i == 8 ? 6 : 7);
    BAR();
    if constexpr (i < 7) dma<ring_slot(i + 3)>(rX, vX, kx, sb3); else x1(IC<i == 7 ? 0 : i == 8 ? 3 : 7>{});
    if (i == 0) { PIN(epv); PIN(pf); tag = rfl(epv) * 1664525u + 1013904223u; STAMP(19, 20); }
#pragma unroll
    for (int ks = 0; ks < 2; ++ks) {
      wf[2 * i + ks][0] = cat(trrd(s, base + aW0 + ks * 12288), trrd(s, base + aW0 + ks * 12288 + 1024));
      xf[i & 1][2 * ks] = lds128(s, base + (ks ? aR1 : aR0));
      wf[2 * i + ks][1] = cat(trrd(s, base + aW1 + ks * 12288), trrd(s, base + aW1 + ks * 12288 + 1024));
      xf[i & 1][2 * ks + 1] = lds128(s, base + (ks ? aR1 : aR0) + 6144);
    }
    SCHED();
    if constexpr (i > 0) mm0(IC<i - 1>{}, 0);
    SCHED();
    if constexpr (i < 7) dma_cont<1024>(rW, vW0, kw); else x1(IC<i == 7 ? 1 : i == 8 ? 4 : 8>{});
    SCHED();
    if constexpr (i > 0) mm0(IC<i - 1>{}, 1);
    SCHED();
    if constexpr (i < 7) dma_cont<2048>(rW, vW1, kw); else x1(IC<i == 7 ? 2 : i == 8 ? 5 : 9>{});
    if constexpr (i == 8) x1(IC<6>{});
  });
  mm0(IC<9>{}, 0); mm0(IC<9>{}, 1);
  STAMP(20, 1);
  const u32 vst = (kq >> 1) * 32768 + l15 * 16 + (kq & 1) * 8, sst = WS_QKV + (ct * 16 + cg * 4) * 32768 + (64 * r + 32 * th) * 16;
  g_epilogue(acc, ws, vst, sst, ct < 5);
  WAITV(8);
  BAR();
  STAMP(1, 2);
  rd1(0); rd1(1); pre(IC<0>{}); SCHED();
  rd1(2); pre(IC<1>{}); mm1(0); SCHED();
  rd1(3); pre(IC<2>{}); mm1(1); SCHED();
  rd1(4); pre(IC<3>{}); mm1(2); SCHED();
  rd1(5); pre(IC<4>{}); mm1(3); SCHED();
  aS0 += 6 * 8192; aS1 += 6 * 8192; PIN(aS0); PIN(aS1);
  mm1(4); SCHED();
  if (w < 4) WAITV(6); else WAITV(5);
  arrive_flag(s, 0, ws, WS_FG + (r * 16 + ct) * 128, tag, lane);
  BAR();
  STAMP(2, 21);
  rd1(6); rd1(7); mm1(5); SCHED();
  rd1(8); mm1(6); SCHED();
  rd1(9); mm1(7); SCHED();
  ld32_sc1(flv, ws, fl0, 0);
  mm1(8); mm1(9);
  STAMP(21, 3);
  g_epilogue(acd, ws, vst, sst + 1024 * 16, ct < 5);
  WAITV(4);
}

template <class Pre>
DEV void r_phase(rsrc_t rX, u64 pwr, u64 pgw, char* s, u32 sb, rsrc_t ws, int w, int lane, int tid, int r,
                 u32& tag, u32 epv, u32 pf, u32& flv, u32 fl0, u32x2& gr, u32& ckey, Pre&& pre STAMP_ARG) {
  const int kq = lane >> 4, l15 = lane & 15;
  const u32 sb1 = sb + w * 1024;
  if ((w & 1) * 1024 + lane * 16 < 1280) {
    if (w < 2) dma<L_RW>(mk_rsrc(pwr, 1280), w * 1024 + lane * 16, 0, sb1);
    else if (w < 4) dma<L_RW + 1536 - 2048>(mk_rsrc(pgw, 1280), (w - 2) * 1024 + lane * 16, 0, sb1);
  }
  u32x4 xr[18], wf[20];
  const u32 v0 = (lane >> 3) * 1280 + (((lane & 7) ^ kq) * 16), v1 = (v0 ^ 64) + 8 * 1280 - 1024, sX = ((w >> 2) * 1024 + 64 * r + 16 * (w & 3)) * 1280;
  static_for<9>([&](auto ic) { constexpr int i = decltype(ic)::v; dma<i * 2048>(rX, v0, sX + i * 128, sb + w * 18432); dma_cont<1024>(rX, v1, sX + i * 128); });
  u32 aw = L_RW + (l15 == 1 ? 1536 : 0) + kq * 16;
  u32 a0 = w * 18432 + l15 * 128 + ((kq ^ (l15 >> 1)) * 16), a1 = a0 ^ 64;
  PIN(aw); PIN(a0); PIN(a1);
  WAITV(18); BAR();
  PIN(epv); PIN(pf); tag = rfl(epv) * 1664525u + 1013904223u;
#pragma unroll
  for (int i = 0; i < 20; ++i) wf[i] = lds128(s, aw + i * 64);
  f32x4 acc = {0.f, 0.f, 0.f, 0.f}, acc2 = acc;
  static_for<3>([&](auto jc) { constexpr int j = decltype(jc)::v;
    SCHED(); WAITV(j == 0 ? 12 : 14 - 6 * j);
#pragma unroll
    for (int i = 3 * j; i < 3 * j + 3; ++i) { xr[2 * i] = lds128(s, a0 + i * 2048); xr[2 * i + 1] = lds128(s, a1 + i * 2048); }
#pragma unroll
    for (int i = 3 * j; i < 3 * j + 3; ++i) { acc = mfma(xr[2 * i], wf[2 * i], acc); acc2 = mfma(xr[2 * i + 1], wf[2 * i + 1], acc2); }
    if (j == 0) { SCHED(); __builtin_amdgcn_s_waitcnt(0xC07F); dma<0>(rX, v0, sX + 1152, sb + w * 18432); dma_cont<1024>(rX, v1, sX + 1152); }
  });
  SCHED(); WAITV(0);
  STAMP(0, 29);
  acc = mfma(lds128(s, a0), wf[18], acc); acc2 = mfma(lds128(s, a1), wf[19], acc2);
  acc += acc2;
  if (l15 < 2) *(f32x4*)(s + L_RB + l15 * 512 + (16 * w + 4 * kq) * 4) = acc;
  BAR();
  STAMP(29, 30);
  if (tid < 128) {
    const int tok = (tid >> 6) * 1024 + 64 * r + (tid & 63);
    const float rv = *(float*)(s + L_RB + tid * 4), gl = *(float*)(s + L_RB + 512 + tid * 4);
    const u32 rb = pk(rv, 0.f) & 0xffffu;
    *(u32*)(s + L_KEYS + tid * 4) = ((rb ^ ((rb & 0x8000u) ? 0xffffu : 0x8000u)) << 16) | (u32)(2047 - tok);
    gr[0] = __float_as_uint(sigmoidf_(gl)); gr[1] = tag;
  }
  BAR();
  u32 key; const u32 rk = rank128(s, tid, key);
  if (rk < 8 && (tid & 3) == 0) { u32x2 c = {key, tag}; st64_sc1(c, ws, WS_CAND + r * 128 + rk * 8); }
  STAMP(30, 3);
  static_for<5>(pre);
  const u32 candoff = WS_CAND + (tid >> 3 & 15) * 128 + (tid & 7) * 8;
  if (w >= 6) { u32x2 cand; ld64_sc1(cand, ws, candoff, 0); WAITV(0); PIN(cand); spin_gran(cand, ws, candoff, 0, tag); ckey = cand[0]; }
  ld32_sc1(flv, ws, fl0, 0);
  WAITV(0);
}
DEV void r_select(char* s, rsrc_t ws, int w, int tid, int r, u32 tag, u32x2 gr, u32 ckey) {
  BAR();
  if (w >= 6) *(u32*)(s + L_KEYS + (tid & 127) * 4) = ckey;
  BAR();
  u32 key; const u32 rk = rank128(s, tid, key);
  const u32x2 c = {key, tag};
  st64_sc1(c, ws, rk < 8 && (tid & 3) == 0 ? WS_SEL + r * 128 + rk * 8 : OOB);
  st64_sc1(gr, ws, tid < 128 ? WS_GG + ((tid >> 6) * 1024 + 64 * r + (tid & 63)) * 8 : OOB);
}

template <u32 BUF> DEV void attn_stage(rsrc_t ws, u32 sb1, u32 so, int lane) {
  dma_sc1<BUF>(ws, lane * 16, so, sb1);
  dma_sc1<BUF + 6144>(ws, lane * 16, so + 32768, sb1);
  dma_sc1<BUF + 12288>(ws, lane * 16, so + 65536, sb1);
  dma_sc1<BUF + 18432>(ws, lane * 16, so + 80 * 32768, sb1);
  dma_sc1<BUF + 18432 + 6272>(ws, lane * 16, so + 81 * 32768, sb1);
  dma_sc1<BUF + 18432 + 12544>(ws, lane * 16, so + 82 * 32768, sb1);
}
DEV float xmax(float m) {
  auto a = __builtin_amdgcn_permlane16_swap(__float_as_uint(m), __float_as_uint(m), false, false);
  m = fmaxf(__uint_as_float(a[0]), __uint_as_float(a[1]));
  auto b = __builtin_amdgcn_permlane32_swap(__float_as_uint(m), __float_as_uint(m), false, false);
  return fmaxf(__uint_as_float(b[0]), __uint_as_float(b[1]));
}
template <class Mid>
DEV void attn_unit(char* s, u32x4 qf, u32 aK, u32 aV0, u32 aV1, int nskip, int lane, rsrc_t ws, u32 vo0, u32 vo1, u32 so, Mid&& mid) {
  const int kq = lane >> 4, l15 = lane & 15, dg = l15 - 4 * kq;
  const float NEG = -__builtin_inff();
  const f32x4 z = {0.f, 0.f, 0.f, 0.f};
  const u32x2 zz = {0u, 0u};
  u32x4 kf[18], va[9], vb[9];
  kf[16] = lds128(s, aK + 16 * 256); kf[0] = lds128(s, aK); kf[1] = lds128(s, aK + 256);
  va[8] = cat(trrd(s, aV0 + 16 * 256), zz); vb[8] = cat(trrd(s, aV1 + 16 * 256), zz);
  kf[2] = lds128(s, aK + 512); kf[3] = lds128(s, aK + 768);
  va[0] = cat(trrd(s, aV0), trrd(s, aV0 + 256)); vb[0] = cat(trrd(s, aV1), trrd(s, aV1 + 256));
  SCHED();
  f32x4 sd = mfma(kf[16], qf, z);
#pragma unroll
  for (int e = 0; e < 4; ++e) sd[e] = e <= dg ? sd[e] : NEG;
  const float m = xmax(fmaxf(fmaxf(sd[0], sd[1]), fmaxf(sd[2], sd[3])));
  const f32x4 c = {-m, -m, -m, -m};
  f32x4 sa = mfma(kf[0], qf, c), sc = mfma(kf[1], qf, c);
  u32x4 P = {pk(ex2(sd[0] - m), ex2(sd[1] - m)), pk(ex2(sd[2] - m), ex2(sd[3] - m)), 0u, 0u};
  f32x4 o0 = mfma(va[8], P, z), o1 = mfma(vb[8], P, z);
#pragma unroll
  for (int e = 0; e < 4; ++e) sa[e] = e > dg ? sa[e] : NEG;
  static_for<8>([&](auto bc) { constexpr int b = decltype(bc)::v;
    SCHED();
    f32x4 na, nc;
    if (b < 7) na = mfma(kf[2 * b + 2], qf, c);
    if (b < 6) { kf[2 * b + 4] = lds128(s, aK + (2 * b + 4) * 256); kf[2 * b + 5] = lds128(s, aK + (2 * b + 5) * 256); }
    P[0] = pk(ex2(sa[0]), ex2(sa[1])); P[1] = pk(ex2(sa[2]), ex2(sa[3]));
    SCHED();
    if (b < 7) nc = mfma(kf[2 * b + 3], qf, c);
    if (b < 7) {
      va[b + 1] = cat(trrd(s, aV0 + (2 * b + 2) * 256), trrd(s, aV0 + (2 * b + 3) * 256));
      vb[b + 1] = cat(trrd(s, aV1 + (2 * b + 2) * 256), trrd(s, aV1 + (2 * b + 3) * 256));
    }
    P[2] = pk(ex2(sc[0]), ex2(sc[1])); P[3] = pk(ex2(sc[2]), ex2(sc[3]));
    SCHED();
    o0 = mfma(va[b], P, o0);
    o1 = mfma(vb[b], P, o1);
    mid(bc);
    sa = na; sc = nc;
  });
  SCHED();
  auto sw = __builtin_amdgcn_permlane16_swap(__float_as_uint(o1[0]), __float_as_uint(o1[0]), false, false);
  float l = kq == 0 ? __uint_as_float(sw[1]) : o1[0];
  {
    const float big = fmaxf(fmaxf(fmaxf(fabsf(o0[0]), fabsf(o0[1])), fmaxf(fabsf(o0[2]), fabsf(o0[3]))),
                            fmaxf(fmaxf(fabsf(o1[0]), fabsf(o1[1])), fmaxf(fabsf(o1[2]), fabsf(o1[3]))));
    const float sum = (o0[0] + o0[1]) + (o0[2] + o0[3]) + (o1[0] + o1[1]) + (o1[2] + o1[3]);
    const bool ok = l < 1.8446744073709552e19f && big < 1e37f && sum == sum;
    if (__builtin_expect(!__all(ok), 0)) {
      float mt = m;
#pragma unroll 1
      for (int t = nskip; t < 16; ++t) {
        f32x4 v = mfma(lds128(s, aK + t * 256), qf, z);
#pragma unroll
        for (int e = 0; e < 4; ++e) mt = fmaxf(mt, (t > 0 || e > dg) ? v[e] : NEG);
      }
      mt = xmax(mt);
      o0 = z; o1 = z;
#pragma unroll 1
      for (int t = nskip; t < 17; ++t) {
        f32x4 v = mfma(lds128(s, aK + t * 256), qf, z);
#pragma unroll
        for (int e = 0; e < 4; ++e) v[e] = (t == 0 ? e > dg : (t < 16 || e <= dg)) ? ex2(v[e] - mt) : 0.f;
        const u32x4 Q = {pk(v[0], v[1]), pk(v[2], v[3]), 0u, 0u};
        o0 = mfma(cat(trrd(s, aV0 + t * 256), zz), Q, o0);
        o1 = mfma(cat(trrd(s, aV1 + t * 256), zz), Q, o1);
      }
      auto s2 = __builtin_amdgcn_permlane16_swap(__float_as_uint(o1[0]), __float_as_uint(o1[0]), false, false);
      l = kq == 0 ? __uint_as_float(s2[1]) : o1[0];
    }
  }
  const float inv = __builtin_amdgcn_rcpf(l);
  u32x2 v0 = {pk(o0[0] * inv, o0[1] * inv), pk(o0[2] * inv, o0[3] * inv)};
  u32x2 v1 = {pk(o1[0] * inv, o1[1] * inv), pk(o1[2] * inv, o1[3] * inv)};
  st64_sc1(v0, ws, vo0, so);
  st64_sc1(v1, ws, vo1, so);
}

DEV void o_compute(char* s, u32 a, u32x4 (&bw)[5][5], u32x4 (&of)[5]) {
  f32x4 acc[5] = {};
#pragma unroll
  for (int ks = 0; ks < 5; ++ks)
#pragma unroll
    for (int nt = 0; nt < 5; ++nt) acc[nt] = mfma(bw[ks][nt], of[ks], acc[nt]);
#pragma unroll
  for (int nt = 0; nt < 5; ++nt) *(f32x4*)(s + a + nt * 1088) = acc[nt];
}
DEV void o_final(char* s, u32 a, float g, const float (&b)[8], rsrc_t rO, u32 vo, u32 so) {
  f32x4 lo = *(f32x4*)(s + a), hi = *(f32x4*)(s + a + 256);
#pragma unroll
  for (int q = 1; q < 4; ++q) { lo += *(f32x4*)(s + a + q * 10880); hi += *(f32x4*)(s + a + q * 10880 + 256); }
  u32x4 o = {pk(g * lo[0] + b[0], g * lo[1] + b[1]), pk(g * lo[2] + b[2], g * lo[3] + b[3]),
             pk(g * hi[0] + b[4], g * hi[1] + b[5]), pk(g * hi[2] + b[6], g * hi[3] + b[7])};
  __builtin_amdgcn_raw_buffer_store_b128(o, rO, vo, so, 0);
}

__global__ __launch_bounds__(512, 2) void fused_kernel(u64 px, u64 pw, u64 pws, u64 pwo, u64 pwr, u64 pgw, u64 pwg, u64 pwu, u64 pwd, u64 pout) {
  extern __shared__ __attribute__((aligned(16))) char smem[];
  char* s = smem;
  const int tid = threadIdx.x, lane = tid & 63;
  u32 pf;
  {
    u64 pc; u32 len;
    asm volatile("s_getpc_b64 %0\nfk_code_lo:\n\ts_mov_b32 %1, fk_code_hi-fk_code_lo" : "=s"(pc), "=s"(len));
    asm volatile("buffer_load_dword %0, %1, %2, 0 offen" : "=&v"(pf) : "v"(tid >= 448 ? (tid - 448 + (blockIdx.x >> 3 & 3) * 64) * 128 : OOB), "s"(mk_rsrc(pc, len)) : "memory");
  }
  const int w = (int)rfl(tid >> 6);
  asm volatile("" : "+s"(px), "+s"(pw), "+s"(pws), "+s"(pwo), "+s"(pwr), "+s"(pgw), "+s"(pwg), "+s"(pwu), "+s"(pwd), "+s"(pout));
  const int b = blockIdx.x, xcd = b & 7, loc = b >> 3;
  const u32 sb = rfl((u32)(size_t)(LDSP(char))smem), sb1 = sb + w * 1024;
  rsrc_t ws = mk_rsrc(pws, WS_END);
  const int kq = lane >> 4, l15 = lane & 15, qp = (lane >> 2) & 3, pp = lane & 3;
  u32 epv; ld32_sc1(epv, ws, WS_EP, 0);
  if (tid < 16) *(u32*)(s + L_CNT + tid * 4) = 0;
  const int r = (xcd >> 1) * 4 + (loc >> 3), ct = (xcd & 1) * 8 + (loc & 7);
  const int h = xcd * 4 + (loc & 3), gp = loc >> 2;
  const int sl = loc & 7, oi = loc >> 3;
  const int rot = ((loc >> 3) * 10 / 4 + (loc & 7) * 10 / 8 + xcd) % 10;
  u32 tag, flv; u32x2 selv;
  rsrc_t rX = mk_rsrc(px, 2048 * 1280), rWo = mk_rsrc(pwo, 640 * 1280);
  u32 vwo[5];
#pragma unroll
  for (int i = 0; i < 5; ++i) { const u32 u = i * 64 + lane, q = (u * 6554) >> 16; vwo[i] = ((q & 19) | (q >> 1 & 4) | (q << 1 & 8)) * 1280 + (u - q * 10) * 16; }
  const u32 swo = w * 40960 + sl * 160, sb5 = sb + w * 5120;
  auto wo_piece = [&](auto gc, auto ic) __attribute__((always_inline)) { constexpr int g = decltype(gc)::v, i = decltype(ic)::v;
    dma<g * 5120 + i * 1024>(rWo, vwo[i], swo + g * 40960, sb5); };
  const int c0 = (5 * h) >> 1, krow = 2 * gp - 4 + w;
  const u32 fcol = ((c0 + 2 * (lane & 1)) >> 4) * 128;
  const u32 flq = WS_FG + (2 * gp + (w >> 2)) * 2048 + fcol, flk = WS_FG + krow * 2048 + (lane & 2 ? 1280 : 640) + fcol;
  const u32 fl1 = (lane < 4 && w < 6) ? flk : flq, fl0 = krow < 0 ? flq : fl1, selo = WS_SEL + r * 128 + (lane & 7) * 8;
  const u64 pfw = w < 2 ? pwg + 0 : pwu + 0;
  u32 tch = 0;
  auto pre = [&](auto ic) __attribute__((always_inline)) {
    if (decltype(ic)::v == 0 && w < 4)
      asm volatile("buffer_load_dword %0, %1, %2, %3 offen" : "+v"(tch) : "v"(lane * 3072), "s"(mk_rsrc(pfw, 640 * 3072)),
                   "s"(((loc % 5) * 128 + (w & 1) * 64) * 3072 + (xcd * 6 + (loc < 30 ? loc / 5 : 0)) * 64) : "memory");
    wo_piece(IC<0>{}, ic); };
  u32x2 gr; u32 ckey;
  if (ct < 15) g_phase(rX, pw, s, sb, ws, w, lane, r, ct, rot, tag, epv, pf, flv, fl0, pre STAMP_PASS);
  else r_phase(rX, pwr, pgw, s, sb, ws, w, lane, tid, r, tag, epv, pf, flv, fl0, gr, ckey, pre STAMP_PASS);

  STAMP(3, 22);
  PIN(flv); spin_flags(flv, ws, fl0, 0, tag);
  STAMP(22, 4);
  const u32 skv = WS_QKV + (80 + c0) * 32768 + krow * 1024;
  const u32 vq = (c0 + (kq > 2 ? 2 : kq)) * 32768 + (gp * 128 + w * 16 + l15) * 16;
  u32x4 q0, q1, xg[4];
  if (w < 6) attn_stage<L_A0>(ws, sb1, krow < 0 ? OOB : skv, lane);
  ld128_sc1(q0, ws, vq, WS_QKV);
  STAMP(4, 23);
  const bool f1 = loc < 30 && w < 4;
  if (ct == 15) r_select(s, ws, w, tid, r, tag, gr, ckey);
  else if (w < 6) WAITV(7); else WAITV(1);
  arrive_flag(s, 1, ws, ct < 15 ? WS_FG + ((16 + r) * 16 + ct) * 128 : OOB, tag, lane);
  STAMP(23, 5);
  const int odd = h & 1;
  const u32x2 ones = {0x3f803f80u, 0x3f803f80u};
  const u32 aone = 18432 + (odd ? 0 : 12544 + 8) + w * 1024 + lane * 16;
  if (ct == 15) WAITV(3); else WAITV(1);
  PIN(tch);
  STAMP(5, 6);
  if (w < 6 && krow >= 0) *(u32x2*)(s + L_A0 + aone) = ones;
  const bool qlo = kq == 3 || (odd && kq == 0), qhi = kq == 3 || (!odd && kq == 2);
  PIN(q0); q0[0] = qlo ? 0u : q0[0]; q0[1] = qlo ? 0u : q0[1]; q0[2] = qhi ? 0u : q0[2]; q0[3] = qhi ? 0u : q0[3];
  const u32 vrow = L_A0 + 18432 + (16 * w + 4 * kq + qp) * 16;
  u32 aK = L_A0 + (kq > 2 ? 2 : kq) * 6144 + w * 256 + l15 * 16;
  u32 aV0 = vrow + ((pp + odd) >> 1) * 6272 + ((pp + odd) & 1) * 8;
  u32 aV1 = vrow + (pp == 0 ? 12544 + odd * 8 : (odd ? 0 : 12544 + 8));
  PIN(aK); PIN(aV0); PIN(aV1);
  u32 vo0 = (c0 + ((kq + odd) >> 1)) * 32768 + ((kq + odd) & 1) * 8 + l15 * 16, vo1 = kq == 0 ? (c0 + 2) * 32768 + odd * 8 + l15 * 16 : OOB;
  const u32 so = WS_O + (gp * 128 + w * 16) * 16;
  PIN(vo0); PIN(vo1);
  const u32 a32 = ((kq >> 1) * 16 + (kq & 1) * 4 + qp) * 32 + pp * 8;
  const int s64 = xcd * 3 + loc / 10, cgp = loc % 10;
  u32x4 fw[4];
  BAR();
  STAMP(6, 7);
  attn_unit(s, q0, aK, aV0, aV1, gp < 2 ? (16 - gp * 8 - w > 0 ? 16 - gp * 8 - w : 0) : 0, lane, ws, vo0, vo1, so, [&](auto bc) { constexpr int b = decltype(bc)::v;
    if constexpr (b < 3) {
      if (w < 4) {
        if (b < 2 && f1) {
          rsrc_t rr = mk_rsrc(pfw, 640 * 3072);
          const u32 vo = ((lane >> 1 & 19) | (lane >> 2 & 4) | (lane & 8)) * 3072 + (lane & 1) * 16;
          const u32 so = ((loc % 5) * 128 + b * 64) * 3072 + (xcd * 6 + loc / 5) * 64 + (w & 1) * 32;
          dma<L_F1W + b * 2048>(rr, vo, so, sb5); dma<L_F1W + b * 2048 + 1024>(rr, vo, so + 32 * 3072, sb5);
        }
      } else if (w < 7) { wo_piece(IC<8>{}, IC<2 * b>{}); if constexpr (b < 2) wo_piece(IC<8>{}, IC<2 * b + 1>{}); }
    }
    if constexpr (b == 7) if (w < 4) { __builtin_amdgcn_s_waitcnt(0xC07F); PIN(fw[0]); PIN(fw[1]); PIN(fw[2]); PIN(fw[3]); wo_piece(IC<8>{}, IC<0>{}); wo_piece(IC<8>{}, IC<1>{}); }
    if constexpr (b == 7) dma<L_F2W>(mk_rsrc(pwd, 1536 * 1280), (lane >> 3) * 1280 + (((lane & 7) ^ (2 * ((kq & 1) | ((w & 1) << 1)))) * 16), (s64 * 64 + 8 * w) * 1280 + cgp * 128, sb1);
    if constexpr (b == 3) { STAMP(7, 24); ld32_sc1(flv, ws, fl1, 16 * 2048); }
    if constexpr (b == 5) {
      STAMP(24, 25);
      WAITV(0); PIN(flv); spin_flags(flv, ws, fl1, 16 * 2048, tag);
      STAMP(25, 26);
      if (w < 6) attn_stage<L_A1>(ws, sb1, skv + 16 * 1024, lane);
      ld128_sc1(q1, ws, vq, WS_QKV + 1024 * 16);
      ld64_sc1(selv, ws, selo, 0);
    }
    if constexpr (b == 6) {
#pragma unroll
      for (int ks = 0; ks < 4; ++ks) fw[ks] = cat(trrd(s, L_F1W + w * 5120 + a32 + ks * 1024), trrd(s, L_F1W + w * 5120 + a32 + ks * 1024 + 256));
    }
  });
  STAMP(26, 8);
  if (w < 4) WAITV(5); else WAITV(3);
  PIN(selv); spin_gran(selv, ws, selo, 0, tag);
  const u32 selkey = selv[0], seltok = 2047 - (selkey & 0xffffu);
  if (f1) {
    const u32 vo = l15 < 8 ? seltok * 1280 + kq * 16 : OOB, so = (loc % 5) * 256;
    ld128<0>(xg[0], rX, vo, so); ld128<64>(xg[1], rX, vo, so); ld128<128>(xg[2], rX, vo, so); ld128<192>(xg[3], rX, vo, so);
  }
  aK += L_A1 - L_A0; aV0 += L_A1 - L_A0; aV1 += L_A1 - L_A0; PIN(aK); PIN(aV0); PIN(aV1);
  if (w < 6) *(u32x2*)(s + L_A1 + aone) = ones;
  PIN(q1); q1[0] = qlo ? 0u : q1[0]; q1[1] = qlo ? 0u : q1[1]; q1[2] = qhi ? 0u : q1[2]; q1[3] = qhi ? 0u : q1[3];
  BAR();
  STAMP(8, 9);
  const bool f2 = loc < 30;
  u32x4 pp1[10];
  const u32 vp1 = WS_P1 + (2 * s64 + (w & 1)) * 10240 + lane * 16;
  auto p1load = [&]() { static_for<10>([&](auto ic) { constexpr int i = decltype(ic)::v; ld128_sc1<(i & 3) * 1024>(pp1[i], ws, vp1, (i >> 2) * 4096); }); };
  const int tokA = 128 * xcd + 32 * oi;
  const u32 fa = WS_FA + (xcd * 32 + (lane & 31)) * 128;
  const int tk = (tid * 6554) >> 16, pc = tid - tk * 10;
  u32x4 of[5], xv0, xv1; u32x2 g0, g1; u32 ef[4];
  const u32 xo = tid < 320 ? (tokA + tk) * 1280 + sl * 160 + pc * 16 : OOB, go = WS_GG + (tokA + (tk & 31)) * 8;
  const u32 eo = (lane & 15) < 15 ? WS_FG + lane * 128 : WS_CAND + kq * 128 + 4, es = (lane & 15) < 15 ? 8192 : 512;
  const bool ew = b == 0 && w == 5;
  attn_unit(s, q1, aK, aV0, aV1, 0, lane, ws, vo0, vo1, so + 1024 * 16, [&](auto bc) { constexpr int b = decltype(bc)::v;
    if constexpr (b == 0) if (w < 4) { wo_piece(IC<8>{}, IC<2>{}); wo_piece(IC<8>{}, IC<3>{}); wo_piece(IC<8>{}, IC<4>{}); }
    if constexpr (b < 2) if (w >= 3) { wo_piece(IC<12>{}, IC<2 * b>{}); wo_piece(IC<12>{}, IC<2 * b + 1>{}); if (b == 1) wo_piece(IC<12>{}, IC<4>{}); }
    if constexpr (b == 2) {
      if (f1) {
        if (w < 3) WAITV(3); else WAITV(8);
        f32x4 c = {0.f, 0.f, 0.f, 0.f};
#pragma unroll
        for (int ks = 0; ks < 4; ++ks) { PIN(xg[ks]); c = mfma(fw[ks], xg[ks], c); }
        const u32 po = WS_P1 + (((xcd * 6 + loc / 5) * 5 + loc % 5) * 2 + (w >> 1)) * 1024 + (w & 1) * 512 + kq * 128 + l15 * 16;
        st128_sc1(gran_pack(c, tag), ws, l15 < 8 ? po : OOB);
      }
    }
    if constexpr (b == 3) {
      if (w >= 4) WAITV(5); else if (!f1) WAITV(3); else if (w < 3) WAITV(8); else WAITV(13);
      arrive_flag(s, 2, ws, WS_FA + (gp * 32 + h) * 128, tag, lane);
      ld128(xv0, rX, xo, 0); ld128(xv1, rX, xo, 1024 * 1280); ld64_sc1(g0, ws, go, 0); ld64_sc1(g1, ws, go, 8192);
    }
    if constexpr (b == 6) {
      if (ew) {
#pragma unroll
        for (int i = 0; i < 4; ++i) ld32_sc1(ef[i], ws, eo + i * es, 0);
      }
    }
    if constexpr (b == 7) { if (w < 2) p1load(); ld32_sc1(flv, ws, fa, 0); }
  });
  STAMP(9, 10);
  WAITV(3);
  const u32 ofo = (w >> 1) * 20 * 32768 + kq * 32768 + (tokA + (w & 1) * 16 + l15) * 16;
  auto ldo0 = [&]() {
#pragma unroll
    for (int ks = 0; ks < 5; ++ks) ld128_sc1(of[ks], ws, ofo, WS_O + ks * 4 * 32768); };
  STAMP(10, 27);
  if (f2 && w < 2) {
#pragma unroll 1
    for (int it = 0; it < SPIN_MAX; ++it) {
      bool ok = true;
#pragma unroll
      for (int i = 0; i < 10; ++i) { PIN(pp1[i]); ok = ok && pp1[i][3] == tag; }
      if (__all(ok)) break;
      p1load(); WAITV(0);
    }
    f32x4 g = gran_unpack(pp1[0]), u = gran_unpack(pp1[1]);
#pragma unroll
    for (int i = 1; i < 5; ++i) { g += gran_unpack(pp1[2 * i]); u += gran_unpack(pp1[2 * i + 1]); }
    u32x2 hv = {pk(g[0] * sigmoidf_(g[0]) * u[0], g[1] * sigmoidf_(g[1]) * u[1]), pk(g[2] * sigmoidf_(g[2]) * u[2], g[3] * sigmoidf_(g[3]) * u[3])};
    *(u32x2*)(s + L_H + (lane & 7) * 128 + (w & 1) * 64 + (lane >> 3) * 8) = hv;
  }
  if (ew) {
#pragma unroll
    for (int i = 0; i < 4; ++i) { PIN(ef[i]); spin_flags(ef[i], ws, eo + i * es, 0, tag); }
    if (lane == 0) st32_sc1(tag, ws, WS_EP);
  }
  BAR();
  STAMP(27, 28);
  const u32 fsw = ((qp >> 1) | ((kq & 1) << 1)) * 32 + pp * 8;
  if (f1) {
    const u32 a = L_F2W + (8 * kq + qp) * 128 + (fsw ^ (w * 32)), ah = L_H + (l15 & 7) * 128 + kq * 16;
    f32x4 c = {0.f, 0.f, 0.f, 0.f};
    c = mfma(cat(trrd(s, a), trrd(s, a + 512)), lds128(s, ah), c);
    c = mfma(cat(trrd(s, a + 4096), trrd(s, a + 4096 + 512)), lds128(s, ah + 64), c);
    st128_sc1(gran_pack(c, tag), ws, l15 < 8 ? WS_Y + ((s64 * 8 + l15) * 160 + cgp * 16 + w * 4 + kq) * 16 : OOB);
  }
  if (f1) WAITV(3); else WAITV(2);
  PIN(flv);
  const bool rdy0 = __all(flv == tag);
  if (rdy0) ldo0();
  u32x4 bw[5][5];
  { u32 a = (w >> 1) * 25600 + ((kq >> 1) * 16 + (kq & 1) * 4 + qp) * 160 + pp * 8;
    PIN(a);
#pragma unroll
    for (int ks = 0; ks < 5; ++ks) {
#pragma unroll
      for (int nt = 0; nt < 5; ++nt) bw[ks][nt] = cat(trrd(s, a + ks * 5120 + nt * 32), trrd(s, a + ks * 5120 + nt * 32 + 1280));
      if (ks == 1) {
        SCHED();
        if (rdy0) { if (f1) WAITV(6); else WAITV(5); } else if (f1) WAITV(1); else WAITV(0);
        arrive_flag(s, 3, ws, WS_FA + ((8 + gp) * 32 + h) * 128, tag, lane);
        if (!rdy0) { u32 t = ~tag; spin_flags(t, ws, fa, 0, tag); ldo0(); }
        STAMP(28, 11);
        SCHED();
      }
    } }
  PIN(g0); PIN(g1); PIN(xv0); PIN(xv1);
  spin_gran(g0, ws, go, 0, tag); spin_gran(g1, ws, go, 8192, tag);
  const float ga = __uint_as_float(g0[0]), gb = __uint_as_float(g1[0]);
  float ba[8], bb[8];
#pragma unroll
  for (int e = 0; e < 4; ++e) {
    ba[2 * e] = (1.f - ga) * blo(xv0[e]); ba[2 * e + 1] = (1.f - ga) * bhi(xv0[e]);
    bb[2 * e] = (1.f - gb) * blo(xv1[e]); bb[2 * e + 1] = (1.f - gb) * bhi(xv1[e]);
  }
  u32 ared = w * 5440 + (lane >> 5) * 544 + (lane & 31) * 16, afin = (tk >> 4) * 5440 + (pc >> 1) * 1088 + (pc & 1) * 544 + (tk & 15) * 16;
  u32 ared1 = ared + 43520, afin1 = afin + 43520;
  PIN(ared); PIN(afin); PIN(ared1); PIN(afin1);
  rsrc_t rO = mk_rsrc(pout, 2048 * 1280);
  const u32 m0 = (u32)__ballot(lane < 8 && ((seltok & 1023u) - (u32)tokA) < 32u);
  u32 my = 0;
  { int k = 5; for (u32 m = m0; m; m &= m - 1) { if (k == w) my |= m & -m; k = k == 7 ? 5 : k + 1; } }
  const int sp = (lane * 3277) >> 16;
  const u32 yb0 = WS_Y + ((sp < 3 ? sp : 0) * 8 * 1280 + sl * 20 + lane - sp * 20) * 16;
  u32x4 y[8];
  auto ldy = [&](int c) {
#pragma unroll
    for (int q = 0; q < 8; ++q) ld128_sc1(y[q], ws, yb0, c * 2560 + q * 20480); };
  if (my) ldy(__builtin_ctz(my));
  BAR();
  STAMP(11, 12);
  if (my) WAITV(8); else if (rdy0) WAITV(1); else WAITV(0);
#pragma unroll
  for (int ks = 0; ks < 5; ++ks) PIN(of[ks]);
  o_compute(s, ared, bw, of);
  ld32_sc1(flv, ws, fa, 256 * 128);
  if (my) WAITV(1);
  for (u32 m = my; m; m &= m - 1) {
    const int c = __builtin_ctz(m);
    if (m != my) { ldy(c); WAITV(0); }
#pragma unroll 1
    for (int it = 0; it < SPIN_MAX; ++it) {
      bool ok = true;
#pragma unroll
      for (int q = 0; q < 8; ++q) { PIN(y[q]); ok = ok && y[q][3] == tag; }
      if (__all(ok)) break;
      ldy(c); WAITV(0);
    }
    f32x4 t = gran_unpack(y[0]);
#pragma unroll
    for (int q = 1; q < 8; ++q) t += gran_unpack(y[q]);
    *(f32x4*)(s + L_Y + c * 1024 + lane * 16) = t;
  }
  STAMP(12, 13);
  BAR();
  for (u32 m = m0; m; m &= m - 1) {
    const int c = __builtin_ctz(m);
    const u32 key = __builtin_amdgcn_readlane(selkey, c), tok = 2047 - (key & 0xffffu);
    if (tid < 320 && (u32)(tokA + tk) == (tok & 1023u)) {
      const u32 o = key >> 16, rb = (o & 0x8000u) ? (o ^ 0x8000u) : (o ^ 0xffffu), a = L_Y + c * 1024 + pc * 32;
      const float rs = sigmoidf_(__uint_as_float(rb << 16));
      f32x4 ya = *(f32x4*)(s + a), yb = *(f32x4*)(s + a + 16);
#pragma unroll
      for (int q = 1; q < 3; ++q) { ya += *(f32x4*)(s + a + q * 320); yb += *(f32x4*)(s + a + q * 320 + 16); }
      const float fa_ = tok < 1024 ? (1.f - ga) * rs : 0.f, fb_ = tok < 1024 ? 0.f : (1.f - gb) * rs;
#pragma unroll
      for (int e = 0; e < 4; ++e) { ba[e] += fa_ * ya[e]; ba[4 + e] += fa_ * yb[e]; bb[e] += fb_ * ya[e]; bb[4 + e] += fb_ * yb[e]; }
    }
  }
  WAITV(0); PIN(flv);
  const bool rdy = __all(flv == tag);
  auto ldo1 = [&]() {
#pragma unroll
    for (int ks = 0; ks < 5; ++ks) ld128_sc1(of[ks], ws, ofo, WS_O + ks * 4 * 32768 + 1024 * 16); };
  if (rdy) { ldo1(); o_final(s, afin, ga, ba, rO, xo, 0); WAITV(1); }
  else { ld32_sc1(flv, ws, fa, 256 * 128); o_final(s, afin, ga, ba, rO, xo, 0); WAITV(1); PIN(flv); spin_flags(flv, ws, fa, 256 * 128, tag); ldo1(); WAITV(0); }
  STAMP(13, 14);
#pragma unroll
  for (int ks = 0; ks < 5; ++ks) PIN(of[ks]);
  STAMP(14, 15);
  o_compute(s, ared1, bw, of);
  BAR();
  STAMP(15, 16);
  if (w < 5) o_final(s, afin1, gb, bb, rO, xo, 1024 * 1280);
  STAMP(16, 17);
  asm volatile("fk_code_hi:" ::: "memory");
}

extern "C" void kernel_launch(void* const* d_in, const int* in_sizes, int n_in, void* d_out, int out_size, void* d_ws, size_t ws_size,
                              hipStream_t stream) {
  (void)hipFuncSetAttribute((const void*)fused_kernel, hipFuncAttributeMaxDynamicSharedMemorySize, LDS_BYTES);
  fused_kernel<<<256, 512, LDS_BYTES, stream>>>((u64)d_in[0], (u64)d_in[1], (u64)d_ws, (u64)d_in[2], (u64)d_in[4], (u64)d_in[8],
                                                (u64)d_in[5], (u64)d_in[6], (u64)d_in[7], (u64)d_out);
}
```

```cpp
#include <hip/hip_runtime.h>
#include <hip/hip_bf16.h>

typedef unsigned short u16;
typedef unsigned int u32;
typedef unsigned long long u64;
typedef __attribute__((ext_vector_type(2))) u32 u32x2;
typedef __attribute__((ext_vector_type(4))) u32 u32x4;
typedef __attribute__((ext_vector_type(4))) float f32x4;
typedef __attribute__((ext_vector_type(8))) __bf16 bf16x8;
typedef __attribute__((ext_vector_type(2))) __bf16 bf16x2;
typedef __attribute__((ext_vector_type(4))) short s16x4;
#define LDSP(T) __attribute__((address_space(3))) T*
typedef __amdgpu_buffer_rsrc_t rsrc_t;
#define DEV __device__ __forceinline__

constexpr u32 WS_QKV = 0;
constexpr u32 WS_O   = WS_QKV + 240u * 32768;
constexpr u32 WS_P1  = WS_O + 80u * 32768;
constexpr u32 WS_Y   = WS_P1 + 480u * 1024;
constexpr u32 WS_FG  = WS_Y + 24u * 8 * 160 * 16;
constexpr u32 WS_FA  = WS_FG + 512u * 128;
constexpr u32 WS_CAND = WS_FA + 512u * 128;
constexpr u32 WS_SEL = WS_CAND + 16u * 128;
constexpr u32 WS_GG  = WS_SEL + 16u * 128;
constexpr u32 WS_EP  = WS_GG + 2048u * 8;
constexpr u32 WS_END = WS_EP + 128;

constexpr u32 L_X1   = 73728;
constexpr u32 L_F1W  = 40960;
constexpr u32 L_A0   = 76800;
constexpr u32 L_A1   = 114048;
constexpr u32 L_F2W  = 151296;
constexpr u32 L_RW   = 155648;
constexpr u32 L_CNT  = 159488;
constexpr u32 L_H    = 159744;
constexpr u32 L_KEYS = 160768;
constexpr u32 L_RB   = 161280;
constexpr u32 L_Y    = 87040;
constexpr u32 LDS_BYTES = 163840;
constexpr int SPIN_MAX = 1 << 14;
constexpr u32 OOB = 0x80000000u;


DEV rsrc_t mk_rsrc(u64 p, u32 bytes) { return __builtin_amdgcn_make_buffer_rsrc((void*)p, 0, bytes, 0x00020000); }
DEV u32 rfl(u32 v) { return (u32)__builtin_amdgcn_readfirstlane((int)v); }
template <u32 L> DEV void dma(rsrc_t r, u32 voff, u32 soff, u32 lds) {
  asm volatile("s_add_u32 m0, %0, %4\n\ts_nop 0\n\tbuffer_load_dwordx4 %1, %2, %3 offen lds" :: "s"(lds), "v"(voff), "s"(r), "s"(soff), "n"(L) : "memory", "scc");
}
template <u32 L> DEV void dma_sc1(rsrc_t r, u32 voff, u32 soff, u32 lds) {
  asm volatile("s_add_u32 m0, %0, %4\n\ts_nop 0\n\tbuffer_load_dwordx4 %1, %2, %3 offen sc1 lds" :: "s"(lds), "v"(voff), "s"(r), "s"(soff), "n"(L) : "memory", "scc");
}
template <u32 L> DEV void dma3(rsrc_t rX, rsrc_t rW, u32 vX, u32 vW0, u32 vW1, u32 sX, u32 sW, u32 lds) {
  asm volatile("s_add_u32 m0, %0, %8\n\ts_nop 0\n\tbuffer_load_dwordx4 %1, %2, %3 offen lds\n\t"
               "buffer_load_dwordx4 %4, %6, %7 offen offset:1024 lds\n\tbuffer_load_dwordx4 %5, %6, %7 offen offset:2048 lds"
               :: "s"(lds), "v"(vX), "s"(rX), "s"(sX), "v"(vW0), "v"(vW1), "s"(rW), "s"(sW), "n"(L) : "memory", "scc");
}
template <u32 I> DEV void dma_cont(rsrc_t r, u32 voff, u32 soff) {
  asm volatile("buffer_load_dwordx4 %0, %1, %2 offen offset:%3 lds" :: "v"(voff), "s"(r), "s"(soff), "n"(I) : "memory");
}
template <u32 I = 0> DEV void ld128_sc1(u32x4& d, rsrc_t r, u32 voff, u32 soff) { asm volatile("buffer_load_dwordx4 %0, %1, %2, %3 offen offset:%4 sc1" : "=&v"(d) : "v"(voff), "s"(r), "s"(soff), "n"(I) : "memory"); }
template <u32 I = 0> DEV void ld128(u32x4& d, rsrc_t r, u32 voff, u32 soff) { asm volatile("buffer_load_dwordx4 %0, %1, %2, %3 offen offset:%4" : "=&v"(d) : "v"(voff), "s"(r), "s"(soff), "n"(I) : "memory"); }
DEV void ld64_sc1(u32x2& d, rsrc_t r, u32 voff, u32 soff) { asm volatile("buffer_load_dwordx2 %0, %1, %2, %3 offen sc1" : "=&v"(d) : "v"(voff), "s"(r), "s"(soff) : "memory"); }
DEV void ld32_sc1(u32& d, rsrc_t r, u32 voff, u32 soff) { asm volatile("buffer_load_dword %0, %1, %2, %3 offen sc1" : "=&v"(d) : "v"(voff), "s"(r), "s"(soff) : "memory"); }
#define WAITV(n) asm volatile("s_waitcnt vmcnt(%0)" :: "n"(n) : "memory")
template <class T> DEV void pin_(T& x) { asm volatile("" : "+v"(x)); }
#define PIN(x) pin_(x)
#define BAR() do { asm volatile("" ::: "memory"); __builtin_amdgcn_s_waitcnt(0xC07F); asm volatile("s_barrier" ::: "memory"); } while (0)
#define SCHED() __builtin_amdgcn_sched_barrier(0)
DEV void st64_sc1(u32x2 v, rsrc_t r, u32 voff, u32 soff = 0) { __builtin_amdgcn_raw_buffer_store_b64(v, r, voff, soff, 16); }
DEV void st128_sc1(u32x4 v, rsrc_t r, u32 voff) { __builtin_amdgcn_raw_buffer_store_b128(v, r, voff, 0, 16); }
DEV void st32_sc1(u32 v, rsrc_t r, u32 voff) { __builtin_amdgcn_raw_buffer_store_b32(v, r, voff, 0, 16); }

DEV u32 pk(float a, float b) { bf16x2 v = {(__bf16)a, (__bf16)b}; return __builtin_bit_cast(u32, v); }
DEV float blo(u32 v) { return __uint_as_float(v << 16); }
DEV float bhi(u32 v) { return __uint_as_float(v & 0xffff0000u); }
DEV f32x4 mfma(u32x4 a, u32x4 b, f32x4 c) { return __builtin_amdgcn_mfma_f32_16x16x32_bf16(__builtin_bit_cast(bf16x8, a), __builtin_bit_cast(bf16x8, b), c, 0, 0, 0); }
DEV u32x2 trrd(char* s, u32 a) { return __builtin_bit_cast(u32x2, __builtin_amdgcn_ds_read_tr16_b64_v4i16((LDSP(s16x4))(s + a))); }
DEV u32x4 lds128(char* s, u32 a) { return *(u32x4*)(s + a); }
DEV u32x4 cat(u32x2 a, u32x2 b) { u32x4 r = {a[0], a[1], b[0], b[1]}; return r; }
DEV float ex2(float v) { return __builtin_amdgcn_exp2f(v); }
DEV float sigmoidf_(float v) { return __builtin_amdgcn_rcpf(1.f + ex2(v * -1.4426950408889634f)); }
DEV u32x4 gran_pack(f32x4 c, u32 tag) {
  const u32 a = __float_as_uint(c[0]), b = __float_as_uint(c[1]), d = __float_as_uint(c[2]), e = __float_as_uint(c[3]);
  u32x4 r = {__builtin_amdgcn_perm(a, e, 0x07060503u), __builtin_amdgcn_perm(b, e, 0x07060502u), __builtin_amdgcn_perm(d, e, 0x07060501u), tag};
  return r;
}
DEV f32x4 gran_unpack(u32x4 g) {
  const u32 t = __builtin_amdgcn_perm(g[0], g[1], 0x04000c0cu);
  f32x4 r = {__uint_as_float(g[0]), __uint_as_float(g[1]), __uint_as_float(g[2]), __uint_as_float(__builtin_amdgcn_perm(t, g[2], 0x0706000cu))};
  return r;
}

DEV void arrive_flag(char* s, int cnt, rsrc_t ws, u32 flagoff, u32 tag, int lane) {
  if (lane == 0) {
    u32 old;
    asm volatile("ds_add_rtn_u32 %0, %1, %2\n\ts_waitcnt lgkmcnt(0)" : "=v"(old) : "v"((u32)(size_t)(LDSP(char))(s + L_CNT + cnt * 4)), "v"(1u) : "memory");
    st32_sc1(tag, ws, old == 7 ? flagoff : OOB);
  }
}
DEV void spin_flags(u32 v, rsrc_t ws, u32 voff, u32 soff, u32 tag) {
#pragma unroll 1
  for (int i = 0; i < SPIN_MAX && !__all(v == tag); ++i) { ld32_sc1(v, ws, voff, soff); WAITV(0); PIN(v); }
}
DEV void spin_gran(u32x2& v, rsrc_t ws, u32 voff, u32 soff, u32 tag) {
#pragma unroll 1
  for (int i = 0; i < SPIN_MAX && !__all(v[1] == tag); ++i) { ld64_sc1(v, ws, voff, soff); WAITV(0); PIN(v); }
}
DEV u32 rank128(char* s, int tid, u32& key) {
  key = *(u32*)(s + L_KEYS + (tid >> 2) * 4);
  u32 c = 0;
#pragma unroll
  for (int i = 0; i < 8; ++i) {
    u32x4 k = lds128(s, L_KEYS + (tid & 3) * 128 + i * 16);
    c += (k[0] > key) + (k[1] > key) + (k[2] > key) + (k[3] > key);
  }
  c += __builtin_amdgcn_update_dpp(0u, c, 0xB1, 0xf, 0xf, true);
  c += __builtin_amdgcn_update_dpp(0u, c, 0x4E, 0xf, 0xf, true);
  return c;
}

DEV void g_epilogue(f32x4 (&acc)[2][2], rsrc_t ws, u32 voff, u32 soff, bool isq) {
  if (isq) {
#pragma unroll
    for (int i = 0; i < 4; ++i) acc[i >> 1][i & 1] *= 0.32259863752095353f;
  }
#pragma unroll
  for (int ft = 0; ft < 2; ++ft) {
    const f32x4 a = acc[ft][0], b = acc[ft][1];
    auto x = __builtin_amdgcn_permlane16_swap(pk(a[0], a[1]), pk(b[0], b[1]), false, false);
    auto y = __builtin_amdgcn_permlane16_swap(pk(a[2], a[3]), pk(b[2], b[3]), false, false);
    const u32x4 v = {x[0], y[0], x[1], y[1]};
    __builtin_amdgcn_raw_buffer_store_b128(v, ws, voff, soff + ft * 65536, 16);
  }
}
constexpr u32 ring_slot(int i) { return (i & 3) == 3 ? L_X1 + 7 * 8192 : (i & 3) * 24576; }
template <int I> struct IC { static constexpr int v = I; };
template <int N, class F> DEV void static_for(F&& f) { if constexpr (N > 0) { static_for<N - 1>(f); f(IC<N - 1>{}); } }

template <class Pre>
DEV void g_phase(rsrc_t rX, u64 pw, char* s, u32 sb, rsrc_t ws, int w, int lane, int r, int ct, int rot,
                 u32& tag, u32 epv, u32 pf, u32& flv, u32& fl0, Pre&& pre) {
  const int cg = w & 3, th = w >> 2, kq = lane >> 4, l15 = lane & 15;
  rsrc_t rW = mk_rsrc(pw - 2048, 640 * 3840 + 2048);
  const u32 vW = kq * 3840 + ((l15 ^ (2 * (kq | ((w & 1) << 2)))) * 16), vW0 = vW + 1024, vW1 = vW + 4 * 3840;
  const u32 vX = (lane >> 3) * 1280 + (((lane & 7) ^ ((4 * w + kq) & 7)) * 16);
  const u32 sW = 8 * w * 3840 + ct * 256, sX = (64 * r + 8 * w) * 1280, sb3 = sb + w * 3072, sb1 = sb + w * 1024;
  u32 k0 = rot * 128, k1 = rot * 128;
  auto batch = [&](auto ic) { constexpr int i = decltype(ic)::v;
    dma3<ring_slot(i)>(rX, rW, vX, vW0, vW1, sX + k0, sW + k0 * 1920, sb3); k0 = k0 == 1152 ? 0 : k0 + 128; };
  auto x1 = [&](auto jc) { constexpr int j = decltype(jc)::v;
    dma<L_X1 + j * 8192>(rX, vX, sX + k1 + 1024 * 1280, sb1); k1 = k1 == 1152 ? 0 : k1 + 128; };
  batch(IC<0>{});
  asm volatile("s_barrier" ::: "memory");
  batch(IC<1>{});
  asm volatile("s_barrier" ::: "memory");
  batch(IC<2>{});
  pre(IC<5>{});
  const int qp = (lane >> 2) & 3, pp = lane & 3, hrow = qp | ((kq & 1) << 2);
  u32 aW0 = kq * 3072 + 1024 + qp * 256 + (((cg * 4 + (pp >> 1)) ^ (2 * hrow)) * 16) + (pp & 1) * 8, aW1 = aW0 ^ 32;
  const u32 xsw = (kq ^ (l15 >> 1)) * 16;
  u32 aR0 = (4 * th + (l15 >> 3)) * 3072 + (l15 & 7) * 128 + xsw, aR1 = aR0 ^ 64;
  u32 aS0 = L_X1 + (32 * th + l15) * 128 + xsw, aS1 = aS0 ^ 64;
  PIN(aW0); PIN(aW1); PIN(aR0); PIN(aR1); PIN(aS0); PIN(aS1);
  u32x4 wf[20][2];
  f32x4 acc[2][2] = {}, acd[2][2] = {};
  u32x4 xf[2][4], xh[10][4];
  auto mm0 = [&](auto ic, int h) { constexpr int i = decltype(ic)::v;
#pragma unroll
    for (int q = 0; q < 4; ++q) acc[q & 1][q >> 1] = mfma(wf[2 * i + h][q & 1], xf[i & 1][2 * h + (q >> 1)], acc[q & 1][q >> 1]); };
  auto rd1 = [&](int j) {
#pragma unroll
    for (int q = 0; q < 4; ++q) xh[j][q] = lds128(s, ((q >> 1) ? aS1 : aS0) + (j < 6 ? j : j - 6) * 8192 + (q & 1) * 2048); };
  auto mm1 = [&](int j) {
#pragma unroll
    for (int q = 0; q < 8; ++q) acd[q & 1][q >> 1 & 1] = mfma(wf[2 * j + (q >> 2)][q & 1], xh[j][q >> 1], acd[q & 1][q >> 1 & 1]); };
  static_for<10>([&](auto ic) { constexpr int i = decltype(ic)::v;
    constexpr u32 base = (i & 3) == 3 ? 0 : ring_slot(i), jump = (i & 3) == 3 ? ring_slot(3) : (i & 3) == 0 && i ? -ring_slot(3) : 0;
    if constexpr (jump != 0) { aW0 += jump; aW1 += jump; aR0 += jump; aR1 += jump; PIN(aW0); PIN(aW1); PIN(aR0); PIN(aR1); }
    const u32 kx = sX + k0, kw = sW + k0 * 1920;
    k0 = k0 == 1152 ? 0 : k0 + 128;
    WAITV(i < 8 ? 6 : i == 8 ? 6 : 7);
    BAR();
    if constexpr (i < 7) dma<ring_slot(i + 3)>(rX, vX, kx, sb3); else x1(IC<i == 7 ? 0 : i == 8 ? 3 : 7>{});
    if (i == 0) { PIN(epv); PIN(pf); tag = rfl(epv) * 1664525u + 1013904223u; }
#pragma unroll
    for (int ks = 0; ks < 2; ++ks) {
      wf[2 * i + ks][0] = cat(trrd(s, base + aW0 + ks * 12288), trrd(s, base + aW0 + ks * 12288 + 1024));
      xf[i & 1][2 * ks] = lds128(s, base + (ks ? aR1 : aR0));
      wf[2 * i + ks][1] = cat(trrd(s, base + aW1 + ks * 12288), trrd(s, base + aW1 + ks * 12288 + 1024));
      xf[i & 1][2 * ks + 1] = lds128(s, base + (ks ? aR1 : aR0) + 6144);
    }
    SCHED();
    if constexpr (i > 0) mm0(IC<i - 1>{}, 0);
    SCHED();
    if constexpr (i < 7) dma_cont<1024>(rW, vW0, kw); else x1(IC<i == 7 ? 1 : i == 8 ? 4 : 8>{});
    SCHED();
    if constexpr (i > 0) mm0(IC<i - 1>{}, 1);
    SCHED();
    if constexpr (i < 7) dma_cont<2048>(rW, vW1, kw); else x1(IC<i == 7 ? 2 : i == 8 ? 5 : 9>{});
    if constexpr (i == 8) x1(IC<6>{});
  });
  mm0(IC<9>{}, 0); mm0(IC<9>{}, 1);
  const u32 vst = (kq >> 1) * 32768 + (lane & 31) * 16, sst = WS_QKV + (ct * 16 + cg * 4) * 32768 + (64 * r + 32 * th) * 16;
  g_epilogue(acc, ws, vst, sst, ct < 5);
  WAITV(6);
  BAR();
  rd1(0); rd1(1); pre(IC<0>{}); SCHED();
  rd1(2); pre(IC<1>{}); mm1(0); SCHED();
  rd1(3); pre(IC<2>{}); mm1(1); SCHED();
  rd1(4); pre(IC<3>{}); mm1(2); SCHED();
  rd1(5); pre(IC<4>{}); mm1(3); SCHED();
  aS0 += 6 * 8192; aS1 += 6 * 8192; PIN(aS0); PIN(aS1);
  mm1(4); SCHED();
  WAITV(5);
  arrive_flag(s, 0, ws, WS_FG + (r * 16 + ct) * 128, tag, lane);
  BAR();
  rd1(6); rd1(7); mm1(5); SCHED();
  rd1(8); mm1(6); SCHED();
  rd1(9); mm1(7); SCHED();
  ld32_sc1(flv, ws, fl0, 0);
  mm1(8); mm1(9);
  g_epilogue(acd, ws, vst, sst + 1024 * 16, ct < 5);
  WAITV(2);
}

template <class Pre>
DEV void r_phase(rsrc_t rX, u64 pwr, u64 pgw, char* s, u32 sb, rsrc_t ws, int w, int lane, int tid, int r,
                 u32& tag, u32 epv, u32 pf, u32& flv, u32& fl0, u32x2& gr, u32& ckey, Pre&& pre) {
  const int kq = lane >> 4, l15 = lane & 15;
  const u32 sb1 = sb + w * 1024;
  if ((w & 1) * 1024 + lane * 16 < 1280) {
    if (w < 2) dma<L_RW>(mk_rsrc(pwr, 1280), w * 1024 + lane * 16, 0, sb1);
    else if (w < 4) dma<L_RW + 1536 - 2048>(mk_rsrc(pgw, 1280), (w - 2) * 1024 + lane * 16, 0, sb1);
  }
  u32x4 xr[18], wf[20];
  const u32 v0 = (lane >> 3) * 1280 + (((lane & 7) ^ kq) * 16), v1 = (v0 ^ 64) + 8 * 1280 - 1024, sX = ((w >> 2) * 1024 + 64 * r + 16 * (w & 3)) * 1280;
  static_for<9>([&](auto ic) { constexpr int i = decltype(ic)::v; dma<i * 2048>(rX, v0, sX + i * 128, sb + w * 18432); dma_cont<1024>(rX, v1, sX + i * 128); });
  pre(IC<5>{});
  u32 aw = L_RW + (l15 == 1 ? 1536 : 0) + kq * 16;
  u32 a0 = w * 18432 + l15 * 128 + ((kq ^ (l15 >> 1)) * 16), a1 = a0 ^ 64;
  PIN(aw); PIN(a0); PIN(a1);
  WAITV(18); BAR();
  PIN(epv); PIN(pf); tag = rfl(epv) * 1664525u + 1013904223u;
#pragma unroll
  for (int i = 0; i < 20; ++i) wf[i] = lds128(s, aw + i * 64);
  f32x4 acc = {0.f, 0.f, 0.f, 0.f}, acc2 = acc;
  static_for<3>([&](auto jc) { constexpr int j = decltype(jc)::v;
    SCHED(); WAITV(j == 0 ? 12 : 14 - 6 * j);
#pragma unroll
    for (int i = 3 * j; i < 3 * j + 3; ++i) { xr[2 * i] = lds128(s, a0 + i * 2048); xr[2 * i + 1] = lds128(s, a1 + i * 2048); }
#pragma unroll
    for (int i = 3 * j; i < 3 * j + 3; ++i) { acc = mfma(xr[2 * i], wf[2 * i], acc); acc2 = mfma(xr[2 * i + 1], wf[2 * i + 1], acc2); }
    if (j == 0) { SCHED(); __builtin_amdgcn_s_waitcnt(0xC07F); dma<0>(rX, v0, sX + 1152, sb + w * 18432); dma_cont<1024>(rX, v1, sX + 1152); }
  });
  SCHED(); WAITV(0);
  acc = mfma(lds128(s, a0), wf[18], acc); acc2 = mfma(lds128(s, a1), wf[19], acc2);
  acc += acc2;
  if (l15 < 2) *(f32x4*)(s + L_RB + l15 * 512 + (16 * w + 4 * kq) * 4) = acc;
  BAR();
  if (tid < 128) {
    const int tok = (tid >> 6) * 1024 + 64 * r + (tid & 63);
    const float rv = *(float*)(s + L_RB + tid * 4), gl = *(float*)(s + L_RB + 512 + tid * 4);
    const u32 rb = pk(rv, 0.f) & 0xffffu;
    *(u32*)(s + L_KEYS + tid * 4) = ((rb ^ ((rb & 0x8000u) ? 0xffffu : 0x8000u)) << 16) | (u32)(2047 - tok);
    gr[0] = __float_as_uint(sigmoidf_(gl)); gr[1] = tag;
  }
  BAR();
  u32 key; const u32 rk = rank128(s, tid, key);
  if (rk < 8 && (tid & 3) == 0) { u32x2 c = {key, tag}; st64_sc1(c, ws, WS_CAND + r * 128 + rk * 8); }
  static_for<5>(pre);
  const u32 candoff = WS_CAND + (tid >> 3 & 15) * 128 + (tid & 7) * 8;
  if (w >= 6) { u32x2 cand; ld64_sc1(cand, ws, candoff, 0); WAITV(0); PIN(cand); spin_gran(cand, ws, candoff, 0, tag); ckey = cand[0]; }
  ld32_sc1(flv, ws, fl0, 0);
  WAITV(0);
}
DEV void r_select(char* s, rsrc_t ws, int w, int tid, int r, u32 tag, u32x2 gr, u32 ckey) {
  BAR();
  if (w >= 6) *(u32*)(s + L_KEYS + (tid & 127) * 4) = ckey;
  BAR();
  u32 key; const u32 rk = rank128(s, tid, key);
  const u32x2 c = {key, tag};
  st64_sc1(c, ws, rk < 8 && (tid & 3) == 0 ? WS_SEL + r * 128 + rk * 8 : OOB);
  st64_sc1(gr, ws, tid < 128 ? WS_GG + ((tid >> 6) * 1024 + 64 * r + (tid & 63)) * 8 : OOB);
}

template <u32 BUF> DEV void attn_stage(rsrc_t ws, u32 sb1, u32 so, int lane) {
  dma_sc1<BUF>(ws, lane * 16, so, sb1);
  dma_sc1<BUF + 6144>(ws, lane * 16, so + 32768, sb1);
  dma_sc1<BUF + 12288>(ws, lane * 16, so + 65536, sb1);
  dma_sc1<BUF + 18432>(ws, lane * 16, so + 80 * 32768, sb1);
  dma_sc1<BUF + 18432 + 6272>(ws, lane * 16, so + 81 * 32768, sb1);
  dma_sc1<BUF + 18432 + 12544>(ws, lane * 16, so + 82 * 32768, sb1);
}
DEV float xmax(float m) {
  auto a = __builtin_amdgcn_permlane16_swap(__float_as_uint(m), __float_as_uint(m), false, false);
  m = fmaxf(__uint_as_float(a[0]), __uint_as_float(a[1]));
  auto b = __builtin_amdgcn_permlane32_swap(__float_as_uint(m), __float_as_uint(m), false, false);
  return fmaxf(__uint_as_float(b[0]), __uint_as_float(b[1]));
}
template <class Mid>
DEV void attn_unit(char* s, u32x4 qf, u32 aK, u32 aV0, u32 aV1, int nskip, int lane, rsrc_t ws, u32 vo0, u32 vo1, u32 so, Mid&& mid) {
  const int kq = lane >> 4, l15 = lane & 15, dg = l15 - 4 * kq;
  const float NEG = -__builtin_inff();
  const f32x4 z = {0.f, 0.f, 0.f, 0.f};
  const u32x2 zz = {0u, 0u};
  u32x4 kf[18], va[9], vb[9];
  kf[16] = lds128(s, aK + 16 * 256); kf[0] = lds128(s, aK); kf[1] = lds128(s, aK + 256);
  va[8] = cat(trrd(s, aV0 + 16 * 256), zz); vb[8] = cat(trrd(s, aV1 + 16 * 256), zz);
  kf[2] = lds128(s, aK + 512); kf[3] = lds128(s, aK + 768);
  va[0] = cat(trrd(s, aV0), trrd(s, aV0 + 256)); vb[0] = cat(trrd(s, aV1), trrd(s, aV1 + 256));
  SCHED();
  f32x4 sd = mfma(kf[16], qf, z);
#pragma unroll
  for (int e = 0; e < 4; ++e) sd[e] = e <= dg ? sd[e] : NEG;
  const float m = xmax(fmaxf(fmaxf(sd[0], sd[1]), fmaxf(sd[2], sd[3])));
  const f32x4 c = {-m, -m, -m, -m};
  f32x4 sa = mfma(kf[0], qf, c), sc = mfma(kf[1], qf, c);
  u32x4 P = {pk(ex2(sd[0] - m), ex2(sd[1] - m)), pk(ex2(sd[2] - m), ex2(sd[3] - m)), 0u, 0u};
  f32x4 o0 = mfma(va[8], P, z), o1 = mfma(vb[8], P, z);
#pragma unroll
  for (int e = 0; e < 4; ++e) sa[e] = e > dg ? sa[e] : NEG;
  static_for<8>([&](auto bc) { constexpr int b = decltype(bc)::v;
    SCHED();
    f32x4 na, nc;
    if (b < 7) na = mfma(kf[2 * b + 2], qf, c);
    if (b < 6) { kf[2 * b + 4] = lds128(s, aK + (2 * b + 4) * 256); kf[2 * b + 5] = lds128(s, aK + (2 * b + 5) * 256); }
    P[0] = pk(ex2(sa[0]), ex2(sa[1])); P[1] = pk(ex2(sa[2]), ex2(sa[3]));
    SCHED();
    if (b < 7) nc = mfma(kf[2 * b + 3], qf, c);
    if (b < 7) {
      va[b + 1] = cat(trrd(s, aV0 + (2 * b + 2) * 256), trrd(s, aV0 + (2 * b + 3) * 256));
      vb[b + 1] = cat(trrd(s, aV1 + (2 * b + 2) * 256), trrd(s, aV1 + (2 * b + 3) * 256));
    }
    P[2] = pk(ex2(sc[0]), ex2(sc[1])); P[3] = pk(ex2(sc[2]), ex2(sc[3]));
    SCHED();
    o0 = mfma(va[b], P, o0);
    o1 = mfma(vb[b], P, o1);
    mid(bc);
    sa = na; sc = nc;
  });
  SCHED();
  auto sw = __builtin_amdgcn_permlane16_swap(__float_as_uint(o1[0]), __float_as_uint(o1[0]), false, false);
  float l = kq == 0 ? __uint_as_float(sw[1]) : o1[0];
  {
    const float big = fmaxf(fmaxf(fmaxf(fabsf(o0[0]), fabsf(o0[1])), fmaxf(fabsf(o0[2]), fabsf(o0[3]))),
                            fmaxf(fmaxf(fabsf(o1[0]), fabsf(o1[1])), fmaxf(fabsf(o1[2]), fabsf(o1[3]))));
    const float sum = (o0[0] + o0[1]) + (o0[2] + o0[3]) + (o1[0] + o1[1]) + (o1[2] + o1[3]);
    const bool ok = l < 1.8446744073709552e19f && big < 1e37f && sum == sum;
    if (__builtin_expect(!__all(ok), 0)) {
      float mt = m;
#pragma unroll 1
      for (int t = nskip; t < 16; ++t) {
        f32x4 v = mfma(lds128(s, aK + t * 256), qf, z);
#pragma unroll
        for (int e = 0; e < 4; ++e) mt = fmaxf(mt, (t > 0 || e > dg) ? v[e] : NEG);
      }
      mt = xmax(mt);
      o0 = z; o1 = z;
#pragma unroll 1
      for (int t = nskip; t < 17; ++t) {
        f32x4 v = mfma(lds128(s, aK + t * 256), qf, z);
#pragma unroll
        for (int e = 0; e < 4; ++e) v[e] = (t == 0 ? e > dg : (t < 16 || e <= dg)) ? ex2(v[e] - mt) : 0.f;
        const u32x4 Q = {pk(v[0], v[1]), pk(v[2], v[3]), 0u, 0u};
        o0 = mfma(cat(trrd(s, aV0 + t * 256), zz), Q, o0);
        o1 = mfma(cat(trrd(s, aV1 + t * 256), zz), Q, o1);
      }
      auto s2 = __builtin_amdgcn_permlane16_swap(__float_as_uint(o1[0]), __float_as_uint(o1[0]), false, false);
      l = kq == 0 ? __uint_as_float(s2[1]) : o1[0];
    }
  }
  const float inv = __builtin_amdgcn_rcpf(l);
  u32x2 v0 = {pk(o0[0] * inv, o0[1] * inv), pk(o0[2] * inv, o0[3] * inv)};
  u32x2 v1 = {pk(o1[0] * inv, o1[1] * inv), pk(o1[2] * inv, o1[3] * inv)};
  st64_sc1(v0, ws, vo0, so);
  st64_sc1(v1, ws, vo1, so);
}

DEV void o_compute(char* s, u32 a, u32x4 (&bw)[5][5], u32x4 (&of)[5]) {
  f32x4 acc[5] = {};
#pragma unroll
  for (int ks = 0; ks < 5; ++ks)
#pragma unroll
    for (int nt = 0; nt < 5; ++nt) acc[nt] = mfma(bw[ks][nt], of[ks], acc[nt]);
#pragma unroll
  for (int nt = 0; nt < 5; ++nt) *(f32x4*)(s + a + nt * 1088) = acc[nt];
}
DEV void o_final(char* s, u32 a, float g, const float (&b)[8], rsrc_t rO, u32 vo, u32 so) {
  f32x4 lo = *(f32x4*)(s + a), hi = *(f32x4*)(s + a + 256);
#pragma unroll
  for (int q = 1; q < 4; ++q) { lo += *(f32x4*)(s + a + q * 10880); hi += *(f32x4*)(s + a + q * 10880 + 256); }
  u32x4 o = {pk(g * lo[0] + b[0], g * lo[1] + b[1]), pk(g * lo[2] + b[2], g * lo[3] + b[3]),
             pk(g * hi[0] + b[4], g * hi[1] + b[5]), pk(g * hi[2] + b[6], g * hi[3] + b[7])};
  __builtin_amdgcn_raw_buffer_store_b128(o, rO, vo, so, 0);
}

__global__ __launch_bounds__(512, 2) void fused_kernel(u64 px, u64 pw, u64 pws, u64 pwo, u64 pwr, u64 pgw, u64 pwg, u64 pwu, u64 pwd, u64 pout) {
  extern __shared__ __attribute__((aligned(16))) char smem[];
  char* s = smem;
  const int tid = threadIdx.x, lane = tid & 63;
  u32 pf;
  {
    u64 pc; u32 len;
    asm volatile("s_getpc_b64 %0\nfk_code_lo:\n\ts_mov_b32 %1, fk_code_hi-fk_code_lo" : "=s"(pc), "=s"(len));
    asm volatile("buffer_load_dword %0, %1, %2, 0 offen" : "=&v"(pf) : "v"(tid >= 448 ? (tid - 448 + (blockIdx.x >> 3 & 3) * 64) * 128 : OOB), "s"(mk_rsrc(pc, len)) : "memory");
  }
  const int w = (int)rfl(tid >> 6);
  asm volatile("" : "+s"(px), "+s"(pw), "+s"(pws), "+s"(pwo), "+s"(pwr), "+s"(pgw), "+s"(pwg), "+s"(pwu), "+s"(pwd), "+s"(pout));
  const u32 b = blockIdx.x & 255, xcd = b & 7, loc = b >> 3;
  const u32 sb = rfl((u32)(size_t)(LDSP(char))smem), sb1 = sb + w * 1024;
  rsrc_t ws = mk_rsrc(pws, WS_END);
  const int kq = lane >> 4, l15 = lane & 15, qp = (lane >> 2) & 3, pp = lane & 3;
  u32 epv; ld32_sc1(epv, ws, WS_EP, 0);
  const int r = (xcd >> 1) * 4 + (loc >> 3), ct = (xcd & 1) * 8 + (loc & 7);
  const int h = xcd * 4 + (loc & 3), gp = loc >> 2;
  const int sl = loc & 7, oi = loc >> 3;
  const u32 rt = (0x7520u >> (loc >> 3) * 4 & 15) + (0x87653210u >> (loc & 7) * 4 & 15) + xcd, rot = rt - (rt >= 10 ? 10 : 0) - (rt >= 20 ? 10 : 0);
  u32 tag, flv; u32x2 selv;
  rsrc_t rX = mk_rsrc(px, 2048 * 1280), rWo = mk_rsrc(pwo, 640 * 1280);
  u32 vwo[5];
  const u32 swo = w * 40960 + sl * 160, sb5 = sb + w * 5120;
  auto wo_piece = [&](auto gc, auto ic) __attribute__((always_inline)) { constexpr int g = decltype(gc)::v, i = decltype(ic)::v;
    dma<g * 5120 + i * 1024>(rWo, vwo[i], swo + g * 40960, sb5); };
  const int c0 = (5 * h) >> 1, krow = 2 * gp - 4 + w;
  u32 fl0, fl1;
  const u64 pfw = w < 2 ? pwg + 0 : pwu + 0;
  auto pre = [&](auto ic) __attribute__((always_inline)) {
    if constexpr (decltype(ic)::v < 5) wo_piece(IC<0>{}, ic);
    else {
      u32 ln = lane; PIN(ln);
#pragma unroll
      for (int i = 0; i < 5; ++i) { const u32 u = i * 64 + ln, q = (u * 6554) >> 16; vwo[i] = ((q & 19) | (q >> 1 & 4) | (q << 1 & 8)) * 1280 + (u - q * 10) * 16; }
      const u32 fcol = ((c0 + 2 * (ln & 1)) >> 4) * 128;
      const u32 flq = WS_FG + (2 * gp + (w >> 2)) * 2048 + fcol, flk = WS_FG + krow * 2048 + (ln & 2 ? 1280 : 640) + fcol;
      fl1 = (ln < 4 && w < 6) ? flk : flq; fl0 = krow < 0 ? flq : fl1;
      if (tid < 16) *(u32*)(s + L_CNT + tid * 4) = 0;
    } };
  u32x2 gr; u32 ckey;
  if (ct < 15) g_phase(rX, pw, s, sb, ws, w, lane, r, ct, rot, tag, epv, pf, flv, fl0, pre);
  else r_phase(rX, pwr, pgw, s, sb, ws, w, lane, tid, r, tag, epv, pf, flv, fl0, gr, ckey, pre);

  PIN(flv); spin_flags(flv, ws, fl0, 0, tag);
  const u32 skv = WS_QKV + (80 + c0) * 32768 + krow * 1024;
  const u32 vq = (c0 + (kq > 2 ? 2 : kq)) * 32768 + (gp * 128 + w * 16 + l15) * 16;
  u32x4 q0, q1, xg[4];
  if (w < 6) attn_stage<L_A0>(ws, sb1, krow < 0 ? OOB : skv, lane);
  ld128_sc1(q0, ws, vq, WS_QKV);
  const bool f1 = loc < 30 && w < 4;
  const u32 selo = WS_SEL + r * 128 + (lane & 7) * 8;
  const int odd = h & 1;
  const u32x2 ones = {0x3f803f80u, 0x3f803f80u};
  const u32 aone = 18432 + (odd ? 0 : 12544 + 8) + w * 1024 + lane * 16;
  const bool qlo = kq == 3 || (odd && kq == 0), qhi = kq == 3 || (!odd && kq == 2);
  const u32 vrow = L_A0 + 18432 + (16 * w + 4 * kq + qp) * 16;
  u32 aK = L_A0 + (kq > 2 ? 2 : kq) * 6144 + w * 256 + l15 * 16;
  u32 aV0 = vrow + ((pp + odd) >> 1) * 6272 + ((pp + odd) & 1) * 8;
  u32 aV1 = vrow + (pp == 0 ? 12544 + odd * 8 : (odd ? 0 : 12544 + 8));
  PIN(aK); PIN(aV0); PIN(aV1);
  u32 vo0 = (c0 + ((kq + odd) >> 1)) * 32768 + ((kq + odd) & 1) * 8 + l15 * 16, vo1 = kq == 0 ? (c0 + 2) * 32768 + odd * 8 + l15 * 16 : OOB;
  const u32 so = WS_O + (gp * 128 + w * 16) * 16;
  PIN(vo0); PIN(vo1);
  const u32 a32 = ((kq >> 1) * 16 + (kq & 1) * 4 + qp) * 32 + pp * 8;
  const int s64 = xcd * 3 + loc / 10, cgp = loc % 10;
  SCHED();
  if (ct == 15) r_select(s, ws, w, tid, r, tag, gr, ckey);
  else if (w < 6) WAITV(7); else WAITV(1);
  arrive_flag(s, 1, ws, ct < 15 ? WS_FG + ((16 + r) * 16 + ct) * 128 : OOB, tag, lane);
  if (ct == 15) WAITV(3); else WAITV(1);
  if (w < 6 && krow >= 0) *(u32x2*)(s + L_A0 + aone) = ones;
  PIN(q0); q0[0] = qlo ? 0u : q0[0]; q0[1] = qlo ? 0u : q0[1]; q0[2] = qhi ? 0u : q0[2]; q0[3] = qhi ? 0u : q0[3];
  u32x4 fw[4]; u32 selkey, seltok;
  BAR();
  attn_unit(s, q0, aK, aV0, aV1, gp < 2 ? (16 - gp * 8 - w > 0 ? 16 - gp * 8 - w : 0) : 0, lane, ws, vo0, vo1, so, [&](auto bc) { constexpr int b = decltype(bc)::v;
    if constexpr (b < 3) {
      if (w < 4) {
        if (b < 2 && f1) {
          rsrc_t rr = mk_rsrc(pfw, 640 * 3072);
          const u32 vo = ((lane >> 1 & 19) | (lane >> 2 & 4) | (lane & 8)) * 3072 + (lane & 1) * 16;
          const u32 so = ((loc % 5) * 128 + b * 64) * 3072 + (xcd * 6 + loc / 5) * 64 + (w & 1) * 32;
          dma<L_F1W + b * 2048>(rr, vo, so, sb5); dma<L_F1W + b * 2048 + 1024>(rr, vo, so + 32 * 3072, sb5);
        }
      } else if (w < 7) { wo_piece(IC<8>{}, IC<2 * b>{}); if constexpr (b < 2) wo_piece(IC<8>{}, IC<2 * b + 1>{}); }
    }
    if constexpr (b == 7) if (w < 4) { __builtin_amdgcn_s_waitcnt(0xC07F); PIN(fw[0]); PIN(fw[1]); PIN(fw[2]); PIN(fw[3]); wo_piece(IC<8>{}, IC<0>{}); wo_piece(IC<8>{}, IC<1>{}); }
    if constexpr (b == 7) dma<L_F2W>(mk_rsrc(pwd, 1536 * 1280), (lane >> 3) * 1280 + (((lane & 7) ^ (2 * ((kq & 1) | ((w & 1) << 1)))) * 16), (s64 * 64 + 8 * w) * 1280 + cgp * 128, sb1);
    if constexpr (b == 3) { ld32_sc1(flv, ws, fl1, 16 * 2048); ld64_sc1(selv, ws, selo, 0); }
    if constexpr (b == 5) {
      WAITV(0); PIN(flv); spin_flags(flv, ws, fl1, 16 * 2048, tag);
      if (w < 6) attn_stage<L_A1>(ws, sb1, skv + 16 * 1024, lane);
      ld128_sc1(q1, ws, vq, WS_QKV + 1024 * 16);
  PIN(selv); spin_gran(selv, ws, selo, 0, tag);
  selkey = selv[0]; seltok = 2047 - (selkey & 0xffffu);
  if (f1) {
    const u32 vo = l15 < 8 ? seltok * 1280 + kq * 16 : OOB, so = (loc % 5) * 256;
    ld128<0>(xg[0], rX, vo, so); ld128<64>(xg[1], rX, vo, so); ld128<128>(xg[2], rX, vo, so); ld128<192>(xg[3], rX, vo, so);
  }
    }
    if constexpr (b == 6) {
#pragma unroll
      for (int ks = 0; ks < 4; ++ks) fw[ks] = cat(trrd(s, L_F1W + w * 5120 + a32 + ks * 1024), trrd(s, L_F1W + w * 5120 + a32 + ks * 1024 + 256));
    }
  });
  if (w < 4) WAITV(5); else WAITV(3);
  aK += L_A1 - L_A0; aV0 += L_A1 - L_A0; aV1 += L_A1 - L_A0; PIN(aK); PIN(aV0); PIN(aV1);
  if (w < 6) *(u32x2*)(s + L_A1 + aone) = ones;
  PIN(q1); q1[0] = qlo ? 0u : q1[0]; q1[1] = qlo ? 0u : q1[1]; q1[2] = qhi ? 0u : q1[2]; q1[3] = qhi ? 0u : q1[3];
  BAR();
  const bool f2 = loc < 30;
  u32x4 pp1[10];
  const u32 vp1 = WS_P1 + (2 * s64 + (w & 1)) * 10240 + lane * 16;
  auto p1load = [&]() { static_for<10>([&](auto ic) { constexpr int i = decltype(ic)::v; ld128_sc1<(i & 3) * 1024>(pp1[i], ws, vp1, (i >> 2) * 4096); }); };
  const int tokA = 128 * xcd + 32 * oi;
  const u32 fa = WS_FA + (xcd * 32 + (lane & 31)) * 128;
  const int tk = (tid * 6554) >> 16, pc = tid - tk * 10;
  u32x4 of[5], xv0, xv1; u32x2 g0, g1; u32 ef[4];
  const u32 xo = tid < 320 ? (tokA + tk) * 1280 + sl * 160 + pc * 16 : OOB, go = WS_GG + (tokA + (tk & 31)) * 8;
  const u32 eo = (lane & 15) < 15 ? WS_FG + lane * 128 : WS_CAND + kq * 128 + 4, es = (lane & 15) < 15 ? 8192 : 512;
  const bool ew = b == 0 && w == 5;
  attn_unit(s, q1, aK, aV0, aV1, 0, lane, ws, vo0, vo1, so + 1024 * 16, [&](auto bc) { constexpr int b = decltype(bc)::v;
    if constexpr (b == 0) {
      if (f1) {
        f32x4 c = {0.f, 0.f, 0.f, 0.f};
#pragma unroll
        for (int ks = 0; ks < 4; ++ks) { PIN(xg[ks]); c = mfma(fw[ks], xg[ks], c); }
        const u32 po = WS_P1 + (((xcd * 6 + loc / 5) * 5 + loc % 5) * 2 + (w >> 1)) * 1024 + (w & 1) * 512 + kq * 128 + l15 * 16;
        st128_sc1(gran_pack(c, tag), ws, l15 < 8 ? po : OOB);
      }
    }
    if constexpr (b == 0) if (w < 4) { wo_piece(IC<8>{}, IC<2>{}); wo_piece(IC<8>{}, IC<3>{}); wo_piece(IC<8>{}, IC<4>{}); }
    if constexpr (b < 2) if (w >= 3) { wo_piece(IC<12>{}, IC<2 * b>{}); wo_piece(IC<12>{}, IC<2 * b + 1>{}); if (b == 1) wo_piece(IC<12>{}, IC<4>{}); }
    if constexpr (b == 2) {
      if (w >= 4) WAITV(5); else if (!f1) WAITV(3); else if (w < 3) WAITV(4); else WAITV(9);
      arrive_flag(s, 2, ws, WS_FA + (gp * 32 + h) * 128, tag, lane);
    }
    if constexpr (b == 3) {
      ld128(xv0, rX, xo, 0); ld128(xv1, rX, xo, 1024 * 1280); ld64_sc1(g0, ws, go, 0); ld64_sc1(g1, ws, go, 8192);
    }
    if constexpr (b == 6) {
      if (ew) {
#pragma unroll
        for (int i = 0; i < 4; ++i) ld32_sc1(ef[i], ws, eo + i * es, 0);
      }
    }
    if constexpr (b == 7) { if (w < 2) p1load(); ld32_sc1(flv, ws, fa, 0); }
  });
  WAITV(3);
  const u32 ofo = (w >> 1) * 20 * 32768 + kq * 32768 + (tokA + (w & 1) * 16 + l15) * 16;
  auto ldo0 = [&]() {
#pragma unroll
    for (int ks = 0; ks < 5; ++ks) ld128_sc1(of[ks], ws, ofo, WS_O + ks * 4 * 32768); };
  u32x4 bw[5][5];
  u32 abw = (w >> 1) * 25600 + ((kq >> 1) * 16 + (kq & 1) * 4 + qp) * 160 + pp * 8;
  PIN(abw);
  auto rdbw = [&](int ks) __attribute__((always_inline)) {
#pragma unroll
    for (int nt = 0; nt < 5; ++nt) bw[ks][nt] = cat(trrd(s, abw + ks * 5120 + nt * 32), trrd(s, abw + ks * 5120 + nt * 32 + 1280)); };
  if (w < 4) { rdbw(0); rdbw(1); rdbw(2); }
  if (f2 && w < 2) {
#pragma unroll 1
    for (int it = 0; it < SPIN_MAX; ++it) {
      bool ok = true;
#pragma unroll
      for (int i = 0; i < 10; ++i) { PIN(pp1[i]); ok = ok && pp1[i][3] == tag; }
      if (__all(ok)) break;
      p1load(); WAITV(0);
    }
    f32x4 g = gran_unpack(pp1[0]), u = gran_unpack(pp1[1]);
#pragma unroll
    for (int i = 1; i < 5; ++i) { g += gran_unpack(pp1[2 * i]); u += gran_unpack(pp1[2 * i + 1]); }
    u32x2 hv = {pk(g[0] * sigmoidf_(g[0]) * u[0], g[1] * sigmoidf_(g[1]) * u[1]), pk(g[2] * sigmoidf_(g[2]) * u[2], g[3] * sigmoidf_(g[3]) * u[3])};
    *(u32x2*)(s + L_H + (lane & 7) * 128 + (w & 1) * 64 + (lane >> 3) * 8) = hv;
  }
  if (ew) {
#pragma unroll
    for (int i = 0; i < 4; ++i) { PIN(ef[i]); spin_flags(ef[i], ws, eo + i * es, 0, tag); }
    if (lane == 0) st32_sc1(tag, ws, WS_EP);
  }
  BAR();
  const u32 fsw = ((qp >> 1) | ((kq & 1) << 1)) * 32 + pp * 8;
  if (f1) {
    const u32 a = L_F2W + (8 * kq + qp) * 128 + (fsw ^ (w * 32)), ah = L_H + (l15 & 7) * 128 + kq * 16;
    f32x4 c = {0.f, 0.f, 0.f, 0.f};
    c = mfma(cat(trrd(s, a), trrd(s, a + 512)), lds128(s, ah), c);
    c = mfma(cat(trrd(s, a + 4096), trrd(s, a + 4096 + 512)), lds128(s, ah + 64), c);
    st128_sc1(gran_pack(c, tag), ws, l15 < 8 ? WS_Y + ((s64 * 8 + l15) * 160 + cgp * 16 + w * 4 + kq) * 16 : OOB);
  }
  if (f1) WAITV(3); else WAITV(2);
  PIN(flv);
  const bool rdy0 = __all(flv == tag);
  if (rdy0) ldo0();
  if (w >= 4) rdbw(0);
  SCHED();
  if (rdy0) { if (f1) WAITV(6); else WAITV(5); } else if (f1) WAITV(1); else WAITV(0);
  arrive_flag(s, 3, ws, WS_FA + ((8 + gp) * 32 + h) * 128, tag, lane);
  u32 fl2;
  ld32_sc1(fl2, ws, rdy0 ? OOB : fa, 0);
  SCHED();
  if (w >= 4) { rdbw(1); rdbw(2); }
  rdbw(3); rdbw(4);
  SCHED();
  if (!rdy0) { WAITV(0); PIN(fl2); spin_flags(fl2, ws, fa, 0, tag); ldo0(); }
  PIN(g0); PIN(g1); PIN(xv0); PIN(xv1);
  spin_gran(g0, ws, go, 0, tag); spin_gran(g1, ws, go, 8192, tag);
  const float ga = __uint_as_float(g0[0]), gb = __uint_as_float(g1[0]);
  float ba[8], bb[8];
#pragma unroll
  for (int e = 0; e < 4; ++e) {
    ba[2 * e] = (1.f - ga) * blo(xv0[e]); ba[2 * e + 1] = (1.f - ga) * bhi(xv0[e]);
    bb[2 * e] = (1.f - gb) * blo(xv1[e]); bb[2 * e + 1] = (1.f - gb) * bhi(xv1[e]);
  }
  u32 ared = w * 5440 + (lane >> 5) * 544 + (lane & 31) * 16, afin = (tk >> 4) * 5440 + (pc >> 1) * 1088 + (pc & 1) * 544 + (tk & 15) * 16;
  u32 ared1 = ared + 43520, afin1 = afin + 43520;
  PIN(ared); PIN(afin); PIN(ared1); PIN(afin1);
  rsrc_t rO = mk_rsrc(pout, 2048 * 1280);
  const u32 m0 = (u32)__ballot(lane < 8 && ((seltok & 1023u) - (u32)tokA) < 32u);
  u32 my = 0;
  { int k = 5; for (u32 m = m0; m; m &= m - 1) { if (k == w) my |= m & -m; k = k == 7 ? 5 : k + 1; } }
  const int sp = (lane * 3277) >> 16;
  const u32 yb0 = WS_Y + ((sp < 3 ? sp : 0) * 8 * 1280 + sl * 20 + lane - sp * 20) * 16;
  u32x4 y[8];
  auto ldy = [&](int c) {
#pragma unroll
    for (int q = 0; q < 8; ++q) ld128_sc1(y[q], ws, yb0, c * 2560 + q * 20480); };
  if (my) ldy(__builtin_ctz(my));
  BAR();
  if (my) WAITV(8); else WAITV(0);
  PIN(fl2);
#pragma unroll
  for (int ks = 0; ks < 5; ++ks) PIN(of[ks]);
  o_compute(s, ared, bw, of);
  ld32_sc1(flv, ws, fa, 256 * 128);
  if (my) WAITV(1);
  for (u32 m = my; m; m &= m - 1) {
    const int c = __builtin_ctz(m);
    if (m != my) { ldy(c); WAITV(0); }
#pragma unroll 1
    for (int it = 0; it < SPIN_MAX; ++it) {
      bool ok = true;
#pragma unroll
      for (int q = 0; q < 8; ++q) { PIN(y[q]); ok = ok && y[q][3] == tag; }
      if (__all(ok)) break;
      ldy(c); WAITV(0);
    }
    f32x4 t = gran_unpack(y[0]);
#pragma unroll
    for (int q = 1; q < 8; ++q) t += gran_unpack(y[q]);
    *(f32x4*)(s + L_Y + c * 1024 + lane * 16) = t;
  }
  BAR();
  for (u32 m = m0; m; m &= m - 1) {
    const int c = __builtin_ctz(m);
    const u32 key = __builtin_amdgcn_readlane(selkey, c), tok = 2047 - (key & 0xffffu);
    if (tid < 320 && (u32)(tokA + tk) == (tok & 1023u)) {
      const u32 o = key >> 16, rb = (o & 0x8000u) ? (o ^ 0x8000u) : (o ^ 0xffffu), a = L_Y + c * 1024 + pc * 32;
      const float rs = sigmoidf_(__uint_as_float(rb << 16));
      f32x4 ya = *(f32x4*)(s + a), yb = *(f32x4*)(s + a + 16);
#pragma unroll
      for (int q = 1; q < 3; ++q) { ya += *(f32x4*)(s + a + q * 320); yb += *(f32x4*)(s + a + q * 320 + 16); }
      const float fa_ = tok < 1024 ? (1.f - ga) * rs : 0.f, fb_ = tok < 1024 ? 0.f : (1.f - gb) * rs;
#pragma unroll
      for (int e = 0; e < 4; ++e) { ba[e] += fa_ * ya[e]; ba[4 + e] += fa_ * yb[e]; bb[e] += fb_ * ya[e]; bb[4 + e] += fb_ * yb[e]; }
    }
  }
  WAITV(0); PIN(flv);
  const bool rdy = __all(flv == tag);
  auto ldo1 = [&]() {
#pragma unroll
    for (int ks = 0; ks < 5; ++ks) ld128_sc1(of[ks], ws, ofo, WS_O + ks * 4 * 32768 + 1024 * 16); };
  if (rdy) { ldo1(); o_final(s, afin, ga, ba, rO, xo, 0); WAITV(1); }
  else { ld32_sc1(flv, ws, fa, 256 * 128); o_final(s, afin, ga, ba, rO, xo, 0); WAITV(1); PIN(flv); spin_flags(flv, ws, fa, 256 * 128, tag); ldo1(); WAITV(0); }
#pragma unroll
  for (int ks = 0; ks < 5; ++ks) PIN(of[ks]);
  o_compute(s, ared1, bw, of);
  BAR();
  if (w < 5) o_final(s, afin1, gb, bb, rO, xo, 1024 * 1280);
  asm volatile("fk_code_hi:" ::: "memory");
}

extern "C" void kernel_launch(void* const* d_in, const int* in_sizes, int n_in, void* d_out, int out_size, void* d_ws, size_t ws_size,
                              hipStream_t stream) {
  (void)hipFuncSetAttribute((const void*)fused_kernel, hipFuncAttributeMaxDynamicSharedMemorySize, LDS_BYTES);
  fused_kernel<<<256, 512, LDS_BYTES, stream>>>((u64)d_in[0], (u64)d_in[1], (u64)d_ws, (u64)d_in[2], (u64)d_in[4], (u64)d_in[8],
                                                (u64)d_in[5], (u64)d_in[6], (u64)d_in[7], (u64)d_out);
}
```
